```python
import torch

def _softmax(x, axis=-1):
    return torch.softmax(x, dim=axis)
def _gelu(x):
    return torch.nn.functional.gelu(x, approximate="tanh")
def _silu(x):
    return torch.nn.functional.silu(x)
def _sigmoid(x):
    return torch.sigmoid(x)
def _layernorm(x, axis=-1, eps=1e-5):
    m = x.mean(dim=axis, keepdim=True)
    v = x.var(dim=axis, keepdim=True, unbiased=False)
    return (x - m) / torch.sqrt(v + eps)
def _scatter_add(tgt, idx, src, axis=0):
    return tgt.clone().index_add_(axis, idx, src)
def _segment_sum(src, seg, n):
    out = torch.zeros((n,) + src.shape[1:], dtype=src.dtype)
    return out.index_add_(0, seg, src)
def _pad(x, pw):
    flat = tuple(v for pair in reversed(pw) for v in pair)
    return torch.nn.functional.pad(x, flat)

kernel_name = 'parallel_sum__gqa_block_cached__glu_gated_reglu__decode'

def setup_inputs():

    _w = (lambda a: (lambda t: t.to(torch.bfloat16) if t.is_floating_point() else t)(torch.as_tensor(a)))
    def _A_setup():
        B, Sq, D = 8, 1, 512
        Hq, Hkv, Sk, Dh = 32, 32, 4096, 128
        assert Hq % Hkv == 0, (Hq, Hkv)
        return {
            "x": _w((lambda *s: torch.randn(*s, dtype=torch.bfloat16) * 0.05)(B, Sq, D)),
            "k_cache": _w((lambda *s: torch.randn(*s, dtype=torch.bfloat16) * 1.6)(B, Hkv, Sk, Dh)),
            "v_cache": _w((lambda *s: torch.randn(*s, dtype=torch.bfloat16) * 0.8)(B, Hkv, Sk, Dh)),
            "wq": _w((lambda *s: torch.randn(*s, dtype=torch.bfloat16) * 1.6)(D, Hq * Dh) * (D ** -0.5)),
            "wo": _w((lambda *s: torch.randn(*s, dtype=torch.bfloat16) * 0.8)(Hq * Dh, D) * ((Hq * Dh) ** -0.5)),
        }
    _A_in = _A_setup()

    def _B_setup():
        B, S, D, F = 8, 1, 512, 1376
        return {
            "x": _w((lambda *s: torch.randn(*s, dtype=torch.bfloat16) * 0.05)(B, S, D)),
            "W_gate": _w((lambda *s: torch.randn(*s, dtype=torch.bfloat16) * 0.4)(D, F) * (2.0 / D) ** 0.5),
            "W_up": _w((lambda *s: torch.randn(*s, dtype=torch.bfloat16) * 0.4)(D, F) * (2.0 / D) ** 0.5),
            "W_down": _w((lambda *s: torch.randn(*s, dtype=torch.bfloat16) * 0.4)(F, D) * (1.0 / F) ** 0.5),
        }
    _B_in = _B_setup()

    return {
        "A_x": _A_in["x"],
        "A_k_cache": _A_in["k_cache"],
        "A_v_cache": _A_in["v_cache"],
        "A_wq": _A_in["wq"],
        "A_wo": _A_in["wo"],
        "B_x": _B_in["x"],
        "B_W_gate": _B_in["W_gate"],
        "B_W_up": _B_in["W_up"],
        "B_W_down": _B_in["W_down"],
    }

def reference(A_x, A_k_cache, A_v_cache, A_wq, A_wo, B_x, B_W_gate, B_W_up, B_W_down):
    def _A_ref(x, k_cache, v_cache, wq, wo):
        B, Sq, D = x.shape
        Hq, Dh = 32, 128
        Hkv = k_cache.shape[1]
        rep = Hq // Hkv
        q = (lambda x, *d: x.permute(*d))((x @ wq).reshape(B, Sq, Hq, Dh), 0, 2, 1, 3)
        k = (lambda x, n, axis: torch.repeat_interleave(x, n, dim=axis))(k_cache, rep, axis=1)
        v = (lambda x, n, axis: torch.repeat_interleave(x, n, dim=axis))(v_cache, rep, axis=1)
        scores = torch.einsum("bhqd,bhkd->bhqk", q, k) * (Dh ** -0.5)
        p = _softmax(scores, axis=-1)
        o = torch.einsum("bhqk,bhkd->bhqd", p, v)
        return (lambda x, *d: x.permute(*d))(o, 0, 2, 1, 3).reshape(B, Sq, Hq * Dh) @ wo

    def _B_ref(x, W_gate, W_up, W_down):
        gate = torch.einsum("bsd,df->bsf", x, W_gate)
        up = torch.einsum("bsd,df->bsf", x, W_up)
        gate = gate * (lambda x, dt: x.to(dt))(gate > 0, gate.dtype)
        h = gate * up
        return torch.einsum("bsf,fd->bsd", h, W_down)

    x = A_x
    xn = _layernorm(x, axis=-1)
    return x + _A_ref(xn, A_k_cache, A_v_cache, A_wq, A_wo) + _B_ref(xn, B_W_gate, B_W_up, B_W_down)
```

```cpp
#include <hip/hip_runtime.h>
#include <hip/hip_bf16.h>
#include <stdint.h>

typedef unsigned u32x4 __attribute__((ext_vector_type(4)));
typedef unsigned u32x2 __attribute__((ext_vector_type(2)));
typedef float f32x4 __attribute__((ext_vector_type(4)));
typedef __bf16 bf16x2 __attribute__((ext_vector_type(2)));

#define CFG_NW 8
#define CFG_CK 16

constexpr int NB = 8, DM = 512, NH = 32, SK = 4096, DH = 128, FF = 1376, FS = 43;
constexpr int NW = CFG_NW, NT = NW * 64;
constexpr int CK = CFG_CK;
constexpr int CL = CK / 4;
constexpr int NCH = SK / (CK * NW);
constexpr int RW = DM / NW;
constexpr int QR = RW / 4;
constexpr int FR = RW / 8;
constexpr int OR = DH / NW;
constexpr int DR = (FS + NW - 1) / NW;
constexpr float SCALE = 0.08838834764831845f;
constexpr float LOG2E = 1.4426950408889634f;
static_assert(NCH >= 2 && NCH % 2 == 0, "chunks");

constexpr int L_QP = 0;
constexpr int L_QB = L_QP + NW * 128;
constexpr int L_FP = L_QB + 64;
constexpr int L_REC = L_FP + NW * 128;
constexpr int L_RA = L_REC + NW * 132;
constexpr int L_RB = L_RA + NW * 512;
constexpr int L_END = L_RB + NW * 512;

__device__ __forceinline__ float bfr(float x) { return __bfloat162float(__float2bfloat16(x)); }
__device__ __forceinline__ float bf_lo(unsigned u) { return __builtin_bit_cast(float, u << 16); }
__device__ __forceinline__ float bf_hi(unsigned u) { return __builtin_bit_cast(float, u & 0xffff0000u); }
__device__ __forceinline__ float dot2(unsigned k, unsigned q, float acc) {
  return __builtin_amdgcn_fdot2_f32_bf16(__builtin_bit_cast(bf16x2, k), __builtin_bit_cast(bf16x2, q), acc, false);
}
template <int CTRL> __device__ __forceinline__ float dpp(float x) {
  return __builtin_bit_cast(float, __builtin_amdgcn_mov_dpp(__builtin_bit_cast(int, x), CTRL, 0xf, 0xf, true));
}
constexpr int ROR1 = 0x121, ROR2 = 0x122, ROR4 = 0x124, ROR8 = 0x128;
__device__ __forceinline__ float row16_sum(float x) {
  x += dpp<ROR8>(x); x += dpp<ROR4>(x); x += dpp<ROR2>(x); x += dpp<ROR1>(x); return x;
}
__device__ __forceinline__ float xrow_sum(float x) {
  auto s = __builtin_amdgcn_permlane16_swap(__float_as_uint(x), __float_as_uint(x), false, false);
  x = __uint_as_float(s[0]) + __uint_as_float(s[1]);
  auto t = __builtin_amdgcn_permlane32_swap(__float_as_uint(x), __float_as_uint(x), false, false);
  return __uint_as_float(t[0]) + __uint_as_float(t[1]);
}
__device__ __forceinline__ float xrow_max(float x) {
  auto s = __builtin_amdgcn_permlane16_swap(__float_as_uint(x), __float_as_uint(x), false, false);
  x = fmaxf(__uint_as_float(s[0]), __uint_as_float(s[1]));
  auto t = __builtin_amdgcn_permlane32_swap(__float_as_uint(x), __float_as_uint(x), false, false);
  return fmaxf(__uint_as_float(t[0]), __uint_as_float(t[1]));
}
__device__ __forceinline__ u32x4 ldnt(const void* p) { return __builtin_nontemporal_load(reinterpret_cast<const u32x4*>(p)); }
__device__ __forceinline__ u32x4 ld16(const void* p) { return *reinterpret_cast<const u32x4*>(p); }
#define SCHED_FENCE() __builtin_amdgcn_sched_barrier(0)

__global__ void __launch_bounds__(NT) fused_decode_kernel(
    const uint16_t* __restrict__ x, const uint16_t* __restrict__ kc, const uint16_t* __restrict__ vc,
    const uint16_t* __restrict__ wq, const uint16_t* __restrict__ wo, const uint16_t* __restrict__ wg,
    const uint16_t* __restrict__ wu, const uint16_t* __restrict__ wd, float* __restrict__ pa,
    float* __restrict__ pb) {
  __shared__ __attribute__((aligned(16))) float lds[L_END];
  const int h = blockIdx.x & (NH - 1), b = blockIdx.x >> 5;
  const int tid = threadIdx.x, lane = tid & 63, w = __builtin_amdgcn_readfirstlane(tid >> 6);
  const int r = lane >> 4, c = lane & 15, r8 = lane >> 3, c8 = lane & 7;

  const uint16_t* xb = x + b * DM;
  const u32x4 xall = ld16(xb + 8 * lane);
  unsigned xq[QR / 2], xf[FR / 2];
#pragma unroll
  for (int i = 0; i < QR / 2; ++i) xq[i] = *reinterpret_cast<const unsigned*>(xb + RW * w + QR * r + 2 * i);
#pragma unroll
  for (int i = 0; i < FR / 2; ++i) xf[i] = *reinterpret_cast<const unsigned*>(xb + RW * w + FR * r8 + 2 * i);
  u32x4 wqv[QR];
#pragma unroll
  for (int i = 0; i < QR; ++i) wqv[i] = ld16(wq + (size_t)(RW * w + QR * r + i) * (NH * DH) + h * DH + 8 * c);
  SCHED_FENCE();

  const char* kbase = reinterpret_cast<const char*>(kc) + (size_t)(b * NH + h) * (SK * DH * 2) + (size_t)w * (CK * DH * 2) + lane * 16;
  const char* vbase = reinterpret_cast<const char*>(vc) + (size_t)(b * NH + h) * (SK * DH * 2) + (size_t)w * (CK * DH * 2) + lane * 16;
  constexpr size_t CSTRIDE = (size_t)NW * CK * DH * 2;
  u32x4 KA[CL], VA[CL], KB[CL], VB[CL];
#define ISSUE(KR, VR, ch) do { _Pragma("unroll") for (int i_ = 0; i_ < CL; ++i_) KR[i_] = ldnt(kbase + (size_t)(ch) * CSTRIDE + i_ * 1024); \
                               _Pragma("unroll") for (int i_ = 0; i_ < CL; ++i_) VR[i_] = ldnt(vbase + (size_t)(ch) * CSTRIDE + i_ * 1024); } while (0)
  ISSUE(KA, VA, 0);
  ISSUE(KB, VB, 1);
  SCHED_FENCE();

  float mean_b, den;
  {
    float xs[8];
#pragma unroll
    for (int j = 0; j < 4; ++j) { xs[2 * j] = bf_lo(xall[j]); xs[2 * j + 1] = bf_hi(xall[j]); }
    float s = 0.f;
#pragma unroll
    for (int j = 0; j < 8; ++j) s += xs[j];
    s = xrow_sum(row16_sum(s));
    const float mean = s * (1.0f / DM);
    float v = 0.f;
#pragma unroll
    for (int j = 0; j < 8; ++j) { const float d = xs[j] - mean; v = fmaf(d, d, v); }
    v = xrow_sum(row16_sum(v));
    const float var_b = bfr(v * (1.0f / DM));
    mean_b = bfr(mean);
    den = bfr(sqrtf(bfr(var_b + 1e-5f)));
  }
  {
    float acc[8];
#pragma unroll
    for (int j = 0; j < 8; ++j) acc[j] = 0.f;
#pragma unroll
    for (int i = 0; i < QR; ++i) {
      const float xv = (i & 1) ? bf_hi(xq[i >> 1]) : bf_lo(xq[i >> 1]);
      const float xn = bfr(bfr(xv - mean_b) / den);
#pragma unroll
      for (int j = 0; j < 4; ++j) {
        acc[2 * j] = fmaf(xn, bf_lo(wqv[i][j]), acc[2 * j]);
        acc[2 * j + 1] = fmaf(xn, bf_hi(wqv[i][j]), acc[2 * j + 1]);
      }
    }
#pragma unroll
    for (int j = 0; j < 8; ++j) acc[j] = xrow_sum(acc[j]);
    if (r == 0) {
      *reinterpret_cast<f32x4*>(&lds[L_QP + w * 128 + 8 * c]) = f32x4{acc[0], acc[1], acc[2], acc[3]};
      *reinterpret_cast<f32x4*>(&lds[L_QP + w * 128 + 8 * c + 4]) = f32x4{acc[4], acc[5], acc[6], acc[7]};
    }
  }
  SCHED_FENCE();
  const int c0 = (FS * h) >> 3;
  const int foff = FS * h - 8 * c0;
  const int fch = min(c0 + c8, FF / 8 - 1);
  u32x4 gv[FR], uv[FR];
#pragma unroll
  for (int i = 0; i < FR; ++i) {
    gv[i] = ld16(wg + (size_t)(RW * w + FR * r8 + i) * FF + 8 * fch);
    uv[i] = ld16(wu + (size_t)(RW * w + FR * r8 + i) * FF + 8 * fch);
  }
  SCHED_FENCE();
  __syncthreads();
  if (tid < 128) {
    float s = 0.f;
#pragma unroll
    for (int i = 0; i < NW; ++i) s += lds[L_QP + i * 128 + tid];
    reinterpret_cast<uint16_t*>(&lds[L_QB])[tid] = (uint16_t)(__builtin_bit_cast(unsigned, bfr(s)) >> 16);
  }
  SCHED_FENCE();
  {
    float ag[8], au[8];
#pragma unroll
    for (int j = 0; j < 8; ++j) { ag[j] = 0.f; au[j] = 0.f; }
#pragma unroll
    for (int i = 0; i < FR; ++i) {
      const float xv = (i & 1) ? bf_hi(xf[i >> 1]) : bf_lo(xf[i >> 1]);
      const float xn = bfr(bfr(xv - mean_b) / den);
#pragma unroll
      for (int j = 0; j < 4; ++j) {
        ag[2 * j] = fmaf(xn, bf_lo(gv[i][j]), ag[2 * j]); ag[2 * j + 1] = fmaf(xn, bf_hi(gv[i][j]), ag[2 * j + 1]);
        au[2 * j] = fmaf(xn, bf_lo(uv[i][j]), au[2 * j]); au[2 * j + 1] = fmaf(xn, bf_hi(uv[i][j]), au[2 * j + 1]);
      }
    }
#pragma unroll
    for (int j = 0; j < 8; ++j) {
      ag[j] += dpp<ROR8>(ag[j]); ag[j] = xrow_sum(ag[j]);
      au[j] += dpp<ROR8>(au[j]); au[j] = xrow_sum(au[j]);
    }
    if (r8 == 0) {
      float* fp = &lds[L_FP + w * 128 + 8 * c8];
      *reinterpret_cast<f32x4*>(fp) = f32x4{ag[0], ag[1], ag[2], ag[3]};
      *reinterpret_cast<f32x4*>(fp + 4) = f32x4{ag[4], ag[5], ag[6], ag[7]};
      *reinterpret_cast<f32x4*>(fp + 64) = f32x4{au[0], au[1], au[2], au[3]};
      *reinterpret_cast<f32x4*>(fp + 68) = f32x4{au[4], au[5], au[6], au[7]};
    }
  }
  SCHED_FENCE();
  __syncthreads();
  const u32x4 q = *reinterpret_cast<const u32x4*>(&lds[L_QB + 4 * c]);

  float m = -INFINITY, l = 0.f, acc[8];
#pragma unroll
  for (int j = 0; j < 8; ++j) acc[j] = 0.f;
#define CONSUME(KR, VR) do { \
    float s_[CL]; float mn_ = m; \
    _Pragma("unroll") for (int i_ = 0; i_ < CL; ++i_) { \
      float d_ = dot2(KR[i_][0], q[0], 0.f); d_ = dot2(KR[i_][1], q[1], d_); d_ = dot2(KR[i_][2], q[2], d_); d_ = dot2(KR[i_][3], q[3], d_); \
      d_ = row16_sum(d_); s_[i_] = bfr(bfr(d_) * SCALE) * LOG2E; mn_ = fmaxf(mn_, s_[i_]); } \
    const float al_ = __builtin_amdgcn_exp2f(m - mn_); m = mn_; \
    float ps_ = 0.f; \
    _Pragma("unroll") for (int i_ = 0; i_ < CL; ++i_) { s_[i_] = __builtin_amdgcn_exp2f(s_[i_] - mn_); ps_ += s_[i_]; } \
    l = fmaf(l, al_, ps_); \
    _Pragma("unroll") for (int j_ = 0; j_ < 8; ++j_) acc[j_] *= al_; \
    _Pragma("unroll") for (int i_ = 0; i_ < CL; ++i_) { \
      _Pragma("unroll") for (int j_ = 0; j_ < 4; ++j_) { \
        acc[2 * j_] = fmaf(s_[i_], bf_lo(VR[i_][j_]), acc[2 * j_]); \
        acc[2 * j_ + 1] = fmaf(s_[i_], bf_hi(VR[i_][j_]), acc[2 * j_ + 1]); } } \
  } while (0)

#pragma unroll 1
  for (int it = 0; it < NCH - 2; it += 2) {
    CONSUME(KA, VA);
    SCHED_FENCE();
    ISSUE(KA, VA, it + 2);
    SCHED_FENCE();
    CONSUME(KB, VB);
    SCHED_FENCE();
    ISSUE(KB, VB, it + 3);
    SCHED_FENCE();
  }
  CONSUME(KA, VA);
  SCHED_FENCE();
  u32x4 wov[OR], wdv[DR];
#pragma unroll
  for (int i = 0; i < OR; ++i) wov[i] = ld16(wo + (size_t)(h * DH + OR * w + i) * DM + 8 * lane);
#pragma unroll
  for (int i = 0; i < DR; ++i) wdv[i] = ld16(wd + (size_t)(FS * h + min(w + NW * i, FS - 1)) * DM + 8 * lane);
  SCHED_FENCE();
  CONSUME(KB, VB);
#undef CONSUME
#undef ISSUE

  {
    const float M = xrow_max(m);
    const float wt = __builtin_amdgcn_exp2f(m - M);
    l = xrow_sum(l * wt);
#pragma unroll
    for (int j = 0; j < 8; ++j) acc[j] = xrow_sum(acc[j] * wt);
    if (r == 0) {
      float* rec = &lds[L_REC + w * 132];
      *reinterpret_cast<f32x4*>(rec + 4 + 8 * c) = f32x4{acc[0], acc[1], acc[2], acc[3]};
      *reinterpret_cast<f32x4*>(rec + 8 + 8 * c) = f32x4{acc[4], acc[5], acc[6], acc[7]};
      if (c == 0) { rec[0] = M; rec[1] = l; }
    }
  }
  __syncthreads();
  {
    float M = -INFINITY;
#pragma unroll
    for (int i = 0; i < NW; ++i) M = fmaxf(M, lds[L_REC + i * 132]);
    float L = 0.f;
    float o[OR];
#pragma unroll
    for (int j = 0; j < OR; ++j) o[j] = 0.f;
#pragma unroll
    for (int i = 0; i < NW; ++i) {
      const float* rec = &lds[L_REC + i * 132];
      const float wt = __builtin_amdgcn_exp2f(rec[0] - M);
      L = fmaf(wt, rec[1], L);
#pragma unroll
      for (int j = 0; j < OR; j += 4) {
        const f32x4 t = *reinterpret_cast<const f32x4*>(rec + 4 + OR * w + j);
        o[j] = fmaf(wt, t[0], o[j]); o[j + 1] = fmaf(wt, t[1], o[j + 1]); o[j + 2] = fmaf(wt, t[2], o[j + 2]); o[j + 3] = fmaf(wt, t[3], o[j + 3]);
      }
    }
    const float inv = 1.0f / L;
#pragma unroll
    for (int j = 0; j < OR; ++j) o[j] = bfr(o[j] * inv);
    float a[8];
#pragma unroll
    for (int j = 0; j < 8; ++j) a[j] = 0.f;
#pragma unroll
    for (int i = 0; i < OR; ++i)
#pragma unroll
      for (int j = 0; j < 4; ++j) {
        a[2 * j] = fmaf(o[i], bf_lo(wov[i][j]), a[2 * j]);
        a[2 * j + 1] = fmaf(o[i], bf_hi(wov[i][j]), a[2 * j + 1]);
      }
    *reinterpret_cast<f32x4*>(&lds[L_RA + w * 512 + 8 * lane]) = f32x4{a[0], a[1], a[2], a[3]};
    *reinterpret_cast<f32x4*>(&lds[L_RA + w * 512 + 8 * lane + 4]) = f32x4{a[4], a[5], a[6], a[7]};
    SCHED_FENCE();

    float bb[8];
#pragma unroll
    for (int j = 0; j < 8; ++j) bb[j] = 0.f;
#pragma unroll
    for (int t = 0; t < DR; ++t) {
      const int jr = w + NW * t;
      if (jr < FS) {
        float g = 0.f, u = 0.f;
#pragma unroll
        for (int i = 0; i < NW; ++i) { g += lds[L_FP + i * 128 + foff + jr]; u += lds[L_FP + i * 128 + 64 + foff + jr]; }
        g = bfr(g); u = bfr(u);
        g = g > 0.f ? g : 0.f;
        const float hv = bfr(g * u);
#pragma unroll
        for (int j = 0; j < 4; ++j) {
          bb[2 * j] = fmaf(hv, bf_lo(wdv[t][j]), bb[2 * j]);
          bb[2 * j + 1] = fmaf(hv, bf_hi(wdv[t][j]), bb[2 * j + 1]);
        }
      }
    }
    *reinterpret_cast<f32x4*>(&lds[L_RB + w * 512 + 8 * lane]) = f32x4{bb[0], bb[1], bb[2], bb[3]};
    *reinterpret_cast<f32x4*>(&lds[L_RB + w * 512 + 8 * lane + 4]) = f32x4{bb[4], bb[5], bb[6], bb[7]};
  }
  __syncthreads();
#pragma unroll
  for (int e = tid; e < 1024; e += NT) {
    const int n = e & 511;
    const float* src = &lds[(e < 512 ? L_RA : L_RB) + n];
    float s = 0.f;
#pragma unroll
    for (int i = 0; i < NW; ++i) s += src[i * 512];
    float* dst = (e < 512 ? pa : pb) + (size_t)(b * NH + h) * DM + n;
    *dst = s;
  }
}

__global__ void __launch_bounds__(128) reduce_kernel(const uint16_t* __restrict__ x, const float* __restrict__ pa,
                                                     const float* __restrict__ pb, uint16_t* __restrict__ out) {
  const int idx = blockIdx.x * 128 + threadIdx.x;
  const int b = idx >> 9, n = idx & 511;
  float va[NH], vb[NH];
#pragma unroll
  for (int i = 0; i < NH; ++i) { va[i] = pa[(size_t)(b * NH + i) * DM + n]; vb[i] = pb[(size_t)(b * NH + i) * DM + n]; }
  float sa = 0.f, sb = 0.f;
#pragma unroll
  for (int i = 0; i < NH; ++i) { sa += va[i]; sb += vb[i]; }
  const float xv = __builtin_bit_cast(float, (unsigned)x[idx] << 16);
  const float o = bfr(bfr(xv + bfr(sa)) + bfr(sb));
  out[idx] = (uint16_t)(__builtin_bit_cast(unsigned, o) >> 16);
}

extern "C" void kernel_launch(void* const* d_in, const int* in_sizes, int n_in, void* d_out, int out_size,
                              void* d_ws, size_t ws_size, hipStream_t stream) {
  const uint16_t* x = (const uint16_t*)d_in[0];
  const uint16_t* kc = (const uint16_t*)d_in[1];
  const uint16_t* vc = (const uint16_t*)d_in[2];
  const uint16_t* wq = (const uint16_t*)d_in[3];
  const uint16_t* wo = (const uint16_t*)d_in[4];
  const uint16_t* wg = (const uint16_t*)d_in[6];
  const uint16_t* wu = (const uint16_t*)d_in[7];
  const uint16_t* wd = (const uint16_t*)d_in[8];
  float* pa = (float*)d_ws;
  float* pb = pa + NB * NH * DM;
  fused_decode_kernel<<<NB * NH, NT, 0, stream>>>(x, kc, vc, wq, wo, wg, wu, wd, pa, pb);
  reduce_kernel<<<NB * DM / 128, 128, 0, stream>>>(x, pa, pb, (uint16_t*)d_out);
}
```

```cpp
#include <hip/hip_runtime.h>
#include <hip/hip_bf16.h>
#include <stdint.h>

typedef unsigned u32x4 __attribute__((ext_vector_type(4)));
typedef unsigned u32x2 __attribute__((ext_vector_type(2)));
typedef float f32x4 __attribute__((ext_vector_type(4)));
typedef float f32x2 __attribute__((ext_vector_type(2)));
typedef __bf16 bf16x2 __attribute__((ext_vector_type(2)));

constexpr int NB = 8, DM = 512, NH = 32, SK = 4096, DH = 128, FF = 1376, FS = 43;
constexpr int NW = 8, NT = NW * 64;
constexpr int CK = 16;
constexpr int CL = CK / 4;
constexpr int NCH = SK / (CK * NW);
constexpr int RW = DM / NW;
constexpr int QW = 4;
constexpr int QRW = DM / QW;
constexpr int QR = QRW / 4;
constexpr int FR = RW / 8;
constexpr int OR = DH / NW;
constexpr int DR = (FS + NW - 1) / NW;
constexpr float SCALE = 0.08838834764831845f;
constexpr float LOG2E = 1.4426950408889634f;
static_assert(NCH >= 6 && NCH % 2 == 0 && RW == 64, "config");

constexpr int L_XN = 0;
constexpr int L_QP = L_XN + NW * 32;
constexpr int L_QB = L_QP + NW * 128;
constexpr int L_FP = L_QB + 64;
constexpr int L_REC = L_FP + NW * 128;
constexpr int L_RA = L_REC + NW * 132;
constexpr int L_RB = L_RA + NW * 512;
constexpr int L_END = L_RB + NW * 512;

__device__ __forceinline__ float bfr(float x) { return __bfloat162float(__float2bfloat16(x)); }
__device__ __forceinline__ float bf_lo(unsigned u) { return __builtin_bit_cast(float, u << 16); }
__device__ __forceinline__ float bf_hi(unsigned u) { return __builtin_bit_cast(float, u & 0xffff0000u); }
__device__ __forceinline__ float dot2(unsigned k, unsigned q, float acc) {
  return __builtin_amdgcn_fdot2_f32_bf16(__builtin_bit_cast(bf16x2, k), __builtin_bit_cast(bf16x2, q), acc, false);
}
__device__ __forceinline__ void axpy8(float (&acc)[8], const u32x4& row, unsigned slo, unsigned shi) {
#pragma unroll
  for (int j = 0; j < 4; ++j) { acc[2 * j] = dot2(row[j], slo, acc[2 * j]); acc[2 * j + 1] = dot2(row[j], shi, acc[2 * j + 1]); }
}
template <int CTRL> __device__ __forceinline__ float dpp(float x) {
  return __builtin_bit_cast(float, __builtin_amdgcn_mov_dpp(__builtin_bit_cast(int, x), CTRL, 0xf, 0xf, true));
}
constexpr int ROR1 = 0x121, ROR2 = 0x122, ROR4 = 0x124, ROR8 = 0x128;
__device__ __forceinline__ float row16_sum(float x) {
  x += dpp<ROR8>(x); x += dpp<ROR4>(x); x += dpp<ROR2>(x); x += dpp<ROR1>(x); return x;
}
__device__ __forceinline__ float xrow_sum(float x) {
  auto s = __builtin_amdgcn_permlane16_swap(__float_as_uint(x), __float_as_uint(x), false, false);
  x = __uint_as_float(s[0]) + __uint_as_float(s[1]);
  auto t = __builtin_amdgcn_permlane32_swap(__float_as_uint(x), __float_as_uint(x), false, false);
  return __uint_as_float(t[0]) + __uint_as_float(t[1]);
}
__device__ __forceinline__ float xrow_max(float x) {
  auto s = __builtin_amdgcn_permlane16_swap(__float_as_uint(x), __float_as_uint(x), false, false);
  x = fmaxf(__uint_as_float(s[0]), __uint_as_float(s[1]));
  auto t = __builtin_amdgcn_permlane32_swap(__float_as_uint(x), __float_as_uint(x), false, false);
  return fmaxf(__uint_as_float(t[0]), __uint_as_float(t[1]));
}
__device__ __forceinline__ u32x4 ldnt(const void* p) { return __builtin_nontemporal_load(reinterpret_cast<const u32x4*>(p)); }
__device__ __forceinline__ u32x4 ld16(const void* p) { return *reinterpret_cast<const u32x4*>(p); }
#define SCHED_FENCE() __builtin_amdgcn_sched_barrier(0)
#define LOOP_SYNC()

__global__ void __launch_bounds__(NT) fused_decode_kernel(
    const uint16_t* __restrict__ x, const uint16_t* __restrict__ kc, const uint16_t* __restrict__ vc,
    const uint16_t* __restrict__ wq, const uint16_t* __restrict__ wo, const uint16_t* __restrict__ wg,
    const uint16_t* __restrict__ wu, const uint16_t* __restrict__ wd, float* __restrict__ pa,
    float* __restrict__ pb) {
  __shared__ __attribute__((aligned(16))) float lds[L_END];
  const int h = blockIdx.x & (NH - 1), b = blockIdx.x >> 5;
  const int tid = threadIdx.x, lane = tid & 63, w = __builtin_amdgcn_readfirstlane(tid >> 6);
  const int r = lane >> 4, c = lane & 15, r8 = lane >> 3, c8 = lane & 7;

  const char* kbase = reinterpret_cast<const char*>(kc) + (size_t)(b * NH + h) * (SK * DH * 2) + (size_t)w * (CK * DH * 2);
  const char* vbase = reinterpret_cast<const char*>(vc) + (size_t)(b * NH + h) * (SK * DH * 2) + (size_t)w * (CK * DH * 2);
  const unsigned loff = lane * 16;
  constexpr size_t CSTRIDE = (size_t)NW * CK * DH * 2;
  u32x4 KA[CL], VA[CL], KB[CL], VB[CL];
#define ISSUE(KR, VR, ch) do { _Pragma("unroll") for (int i_ = 0; i_ < CL; ++i_) KR[i_] = ldnt(kbase + (size_t)(ch) * CSTRIDE + i_ * 1024 + loff); \
                               _Pragma("unroll") for (int i_ = 0; i_ < CL; ++i_) VR[i_] = ldnt(vbase + (size_t)(ch) * CSTRIDE + i_ * 1024 + loff); } while (0)
  uint16_t* xn16 = reinterpret_cast<uint16_t*>(&lds[L_XN]);
  if (w < QW) {
    const uint16_t* xb = x + b * DM;
    const u32x4 xall = ld16(xb + 8 * lane);
    const unsigned xm0 = xb[QRW * w + lane], xm1 = xb[QRW * w + 64 + lane];
    u32x4 wqv[QR];
    {
      const char* base = reinterpret_cast<const char*>(wq) + (size_t)(QRW * w) * (NH * DH * 2) + h * (DH * 2);
      const unsigned voff = (unsigned)(QR * r) * (NH * DH * 2) + 16 * c;
#pragma unroll
      for (int i = 0; i < QR; ++i) wqv[i] = ld16(base + (size_t)i * (NH * DH * 2) + voff);
    }
    SCHED_FENCE();
    __builtin_amdgcn_s_barrier();
    SCHED_FENCE();
    {
      float xs[8];
#pragma unroll
      for (int j = 0; j < 4; ++j) { xs[2 * j] = bf_lo(xall[j]); xs[2 * j + 1] = bf_hi(xall[j]); }
      float s = ((xs[0] + xs[1]) + (xs[2] + xs[3])) + ((xs[4] + xs[5]) + (xs[6] + xs[7]));
      s = xrow_sum(row16_sum(s));
      const float mean = s * (1.0f / DM);
      float v = 0.f;
#pragma unroll
      for (int j = 0; j < 8; ++j) { const float d = xs[j] - mean; v = fmaf(d, d, v); }
      v = xrow_sum(row16_sum(v));
      const float var_b = bfr(v * (1.0f / DM));
      const float mean_b = bfr(mean);
      const float den = bfr(sqrtf(bfr(var_b + 1e-5f)));
      const float xn0 = bfr(bfr(bf_lo(xm0) - mean_b) / den);
      const float xn1 = bfr(bfr(bf_lo(xm1) - mean_b) / den);
      xn16[QRW * w + lane] = (uint16_t)(__builtin_bit_cast(unsigned, xn0) >> 16);
      xn16[QRW * w + 64 + lane] = (uint16_t)(__builtin_bit_cast(unsigned, xn1) >> 16);
    }
    u32x4 xa[QR / 8];
#pragma unroll
    for (int i = 0; i < QR / 8; ++i) xa[i] = *reinterpret_cast<const u32x4*>(xn16 + QRW * w + QR * r + 8 * i);
    float acc[8];
#pragma unroll
    for (int j = 0; j < 8; ++j) acc[j] = 0.f;
#pragma unroll
    for (int i = 0; i < QR; ++i) {
      const unsigned p = xa[i >> 3][(i >> 1) & 3];
      axpy8(acc, wqv[i], (i & 1) ? (p >> 16) : (p & 0xffffu), (i & 1) ? (p & 0xffff0000u) : (p << 16));
    }
#pragma unroll
    for (int j = 0; j < 8; ++j) acc[j] = xrow_sum(acc[j]);
    if (r == 0) {
      *reinterpret_cast<f32x4*>(&lds[L_QP + w * 128 + 8 * c]) = f32x4{acc[0], acc[1], acc[2], acc[3]};
      *reinterpret_cast<f32x4*>(&lds[L_QP + w * 128 + 8 * c + 4]) = f32x4{acc[4], acc[5], acc[6], acc[7]};
    }
  } else {
    __builtin_amdgcn_s_barrier();
    SCHED_FENCE();
    ISSUE(KA, VA, 0);
    ISSUE(KB, VB, 1);
  }
  SCHED_FENCE();
  __syncthreads();
  if (tid < 128) {
    float s = 0.f;
#pragma unroll
    for (int i = 0; i < QW; ++i) s += lds[L_QP + i * 128 + tid];
    reinterpret_cast<uint16_t*>(&lds[L_QB])[tid] = (uint16_t)(__builtin_bit_cast(unsigned, bfr(s)) >> 16);
  }
  SCHED_FENCE();
  __syncthreads();
  if (w < QW) {
    ISSUE(KA, VA, 0);
    ISSUE(KB, VB, 1);
  }
  SCHED_FENCE();
  const int c0 = (FS * h) >> 3;
  const int foff = FS * h - 8 * c0;
  u32x4 gv[FR], uv[FR];
  {
    const unsigned voff = (unsigned)(FR * r8) * (FF * 2) + 16 * min(c0 + c8, FF / 8 - 1);
    const char* gb = reinterpret_cast<const char*>(wg) + (size_t)(RW * w) * (FF * 2);
    const char* ub = reinterpret_cast<const char*>(wu) + (size_t)(RW * w) * (FF * 2);
#pragma unroll
    for (int i = 0; i < FR; ++i) { gv[i] = ld16(gb + (size_t)i * (FF * 2) + voff); uv[i] = ld16(ub + (size_t)i * (FF * 2) + voff); }
  }
  SCHED_FENCE();
  const u32x4 q = *reinterpret_cast<const u32x4*>(&lds[L_QB + 4 * c]);

  float m = -INFINITY, l = 0.f, acc[8];
#pragma unroll
  for (int j = 0; j < 8; ++j) acc[j] = 0.f;
#define CONSUME(KR, VR) do { \
    float s_[CL]; float mn_ = m; \
    _Pragma("unroll") for (int i_ = 0; i_ < CL; ++i_) { \
      float d_ = dot2(KR[i_][0], q[0], 0.f); d_ = dot2(KR[i_][1], q[1], d_); d_ = dot2(KR[i_][2], q[2], d_); d_ = dot2(KR[i_][3], q[3], d_); \
      d_ = row16_sum(d_); s_[i_] = bfr(bfr(d_) * SCALE) * LOG2E; mn_ = fmaxf(mn_, s_[i_]); } \
    const float al_ = __builtin_amdgcn_exp2f(m - mn_); m = mn_; \
    float ps_ = 0.f; \
    _Pragma("unroll") for (int i_ = 0; i_ < CL; ++i_) { s_[i_] = __builtin_amdgcn_exp2f(s_[i_] - mn_); ps_ += s_[i_]; } \
    l = fmaf(l, al_, ps_); \
    _Pragma("unroll") for (int j_ = 0; j_ < 8; ++j_) acc[j_] *= al_; \
    _Pragma("unroll") for (int i_ = 0; i_ < CL; ++i_) { \
      _Pragma("unroll") for (int j_ = 0; j_ < 4; ++j_) { \
        acc[2 * j_] = fmaf(s_[i_], bf_lo(VR[i_][j_]), acc[2 * j_]); \
        acc[2 * j_ + 1] = fmaf(s_[i_], bf_hi(VR[i_][j_]), acc[2 * j_ + 1]); } } \
    asm volatile("" : "+v"(acc[0]), "+v"(acc[1]), "+v"(acc[2]), "+v"(acc[3]), "+v"(acc[4]), "+v"(acc[5]), "+v"(acc[6]), "+v"(acc[7]), "+v"(l), "+v"(m)); \
  } while (0)
#define STEP2(it) do { \
    CONSUME(KA, VA); SCHED_FENCE(); ISSUE(KA, VA, (it) + 2); SCHED_FENCE(); \
    CONSUME(KB, VB); SCHED_FENCE(); ISSUE(KB, VB, (it) + 3); SCHED_FENCE(); } while (0)

  STEP2(0);
  {
    const u32x4 xa = *reinterpret_cast<const u32x4*>(xn16 + RW * w + FR * r8);
    float ag[8], au[8];
#pragma unroll
    for (int j = 0; j < 8; ++j) { ag[j] = 0.f; au[j] = 0.f; }
#pragma unroll
    for (int i = 0; i < FR; ++i) {
      const unsigned p = xa[i >> 1];
      const unsigned slo = (i & 1) ? (p >> 16) : (p & 0xffffu), shi = (i & 1) ? (p & 0xffff0000u) : (p << 16);
      axpy8(ag, gv[i], slo, shi);
      axpy8(au, uv[i], slo, shi);
    }
#pragma unroll
    for (int j = 0; j < 8; ++j) {
      ag[j] += dpp<ROR8>(ag[j]); ag[j] = xrow_sum(ag[j]);
      au[j] += dpp<ROR8>(au[j]); au[j] = xrow_sum(au[j]);
    }
    if (r8 == 0) {
      float* fp = &lds[L_FP + w * 128 + 8 * c8];
      *reinterpret_cast<f32x4*>(fp) = f32x4{ag[0], ag[1], ag[2], ag[3]};
      *reinterpret_cast<f32x4*>(fp + 4) = f32x4{ag[4], ag[5], ag[6], ag[7]};
      *reinterpret_cast<f32x4*>(fp + 64) = f32x4{au[0], au[1], au[2], au[3]};
      *reinterpret_cast<f32x4*>(fp + 68) = f32x4{au[4], au[5], au[6], au[7]};
    }
  }
  SCHED_FENCE();
  u32x4 wov[OR], wdv[DR];
  {
    const char* ob = reinterpret_cast<const char*>(wo) + (size_t)(h * DH + OR * w) * (DM * 2);
    const char* db = reinterpret_cast<const char*>(wd) + (size_t)(FS * h) * (DM * 2);
#pragma unroll
    for (int i = 0; i < OR; ++i) wov[i] = ld16(ob + i * (DM * 2) + loff);
#pragma unroll
    for (int i = 0; i < DR; ++i) wdv[i] = ld16(db + (size_t)min(w + NW * i, FS - 1) * (DM * 2) + loff);
  }
  SCHED_FENCE();
#pragma unroll 1
  for (int it = 2; it < NCH - 2; it += 2) { STEP2(it); LOOP_SYNC(); }
  CONSUME(KA, VA);
  SCHED_FENCE();
  CONSUME(KB, VB);
#undef STEP2
#undef CONSUME
#undef ISSUE

  {
    const float M = xrow_max(m);
    const float wt = __builtin_amdgcn_exp2f(m - M);
    l = xrow_sum(l * wt);
#pragma unroll
    for (int j = 0; j < 8; ++j) acc[j] = xrow_sum(acc[j] * wt);
    if (r == 0) {
      float* rec = &lds[L_REC + w * 132];
      *reinterpret_cast<f32x4*>(rec + 4 + 8 * c) = f32x4{acc[0], acc[1], acc[2], acc[3]};
      *reinterpret_cast<f32x4*>(rec + 8 + 8 * c) = f32x4{acc[4], acc[5], acc[6], acc[7]};
      if (c == 0) *reinterpret_cast<f32x2*>(rec) = f32x2{M, l};
    }
  }
  SCHED_FENCE();
  __syncthreads();
  SCHED_FENCE();
  {
    f32x2 ml[NW];
    float av[NW], g = 0.f, u = 0.f;
    const int jr = min(w + NW * (lane & 7), FS - 1);
#pragma unroll
    for (int i = 0; i < NW; ++i) {
      ml[i] = *reinterpret_cast<const f32x2*>(&lds[L_REC + i * 132]);
      av[i] = lds[L_REC + i * 132 + 4 + OR * w + c];
      g += lds[L_FP + i * 128 + foff + jr];
      u += lds[L_FP + i * 128 + 64 + foff + jr];
    }
    float M = ml[0][0];
#pragma unroll
    for (int i = 1; i < NW; ++i) M = fmaxf(M, ml[i][0]);
    float L = 0.f, o = 0.f;
#pragma unroll
    for (int i = 0; i < NW; ++i) {
      const float wt = __builtin_amdgcn_exp2f(ml[i][0] - M);
      L = fmaf(wt, ml[i][1], L);
      o = fmaf(wt, av[i], o);
    }
    const unsigned ob = __builtin_bit_cast(unsigned, bfr(o / L));
    g = bfr(g); u = bfr(u);
    g = g > 0.f ? g : 0.f;
    const unsigned hb = __builtin_bit_cast(unsigned, bfr(g * u));
    float a[8], bb[8];
#pragma unroll
    for (int j = 0; j < 8; ++j) { a[j] = 0.f; bb[j] = 0.f; }
#pragma unroll
    for (int i = 0; i < OR; ++i) {
      const unsigned s = __builtin_amdgcn_readlane(ob, i);
      axpy8(a, wov[i], s >> 16, s);
    }
#pragma unroll
    for (int t = 0; t < DR; ++t) {
      unsigned s = __builtin_amdgcn_readlane(hb, t);
      if (w + NW * t >= FS) s = 0u;
      axpy8(bb, wdv[t], s >> 16, s);
    }
    *reinterpret_cast<f32x4*>(&lds[L_RA + w * 512 + 8 * lane]) = f32x4{a[0], a[1], a[2], a[3]};
    *reinterpret_cast<f32x4*>(&lds[L_RA + w * 512 + 8 * lane + 4]) = f32x4{a[4], a[5], a[6], a[7]};
    *reinterpret_cast<f32x4*>(&lds[L_RB + w * 512 + 8 * lane]) = f32x4{bb[0], bb[1], bb[2], bb[3]};
    *reinterpret_cast<f32x4*>(&lds[L_RB + w * 512 + 8 * lane + 4]) = f32x4{bb[4], bb[5], bb[6], bb[7]};
  }
  __syncthreads();
  {
    float sa = 0.f, sb = 0.f;
#pragma unroll
    for (int i = 0; i < NW; ++i) { sa += lds[L_RA + i * 512 + tid]; sb += lds[L_RB + i * 512 + tid]; }
    pa[(size_t)(b * NH + h) * DM + tid] = sa;
    pb[(size_t)(b * NH + h) * DM + tid] = sb;
  }
}

__global__ void __launch_bounds__(128) reduce_kernel(const uint16_t* __restrict__ x, const float* __restrict__ pa,
                                                     const float* __restrict__ pb, uint16_t* __restrict__ out) {
  const int idx = blockIdx.x * 128 + threadIdx.x;
  const int b = idx >> 9, n = idx & 511;
  float va[NH], vb[NH];
#pragma unroll
  for (int i = 0; i < NH; ++i) { va[i] = pa[(size_t)(b * NH + i) * DM + n]; vb[i] = pb[(size_t)(b * NH + i) * DM + n]; }
  float sa = 0.f, sb = 0.f;
#pragma unroll
  for (int i = 0; i < NH; ++i) { sa += va[i]; sb += vb[i]; }
  const float xv = __builtin_bit_cast(float, (unsigned)x[idx] << 16);
  const float o = bfr(bfr(xv + bfr(sa)) + bfr(sb));
  out[idx] = (uint16_t)(__builtin_bit_cast(unsigned, o) >> 16);
}

extern "C" void kernel_launch(void* const* d_in, const int* in_sizes, int n_in, void* d_out, int out_size,
                              void* d_ws, size_t ws_size, hipStream_t stream) {
  const uint16_t* x = (const uint16_t*)d_in[0];
  const uint16_t* kc = (const uint16_t*)d_in[1];
  const uint16_t* vc = (const uint16_t*)d_in[2];
  const uint16_t* wq = (const uint16_t*)d_in[3];
  const uint16_t* wo = (const uint16_t*)d_in[4];
  const uint16_t* wg = (const uint16_t*)d_in[6];
  const uint16_t* wu = (const uint16_t*)d_in[7];
  const uint16_t* wd = (const uint16_t*)d_in[8];
  float* pa = (float*)d_ws;
  float* pb = pa + NB * NH * DM;
  fused_decode_kernel<<<NB * NH, NT, 0, stream>>>(x, kc, vc, wq, wo, wg, wu, wd, pa, pb);
  reduce_kernel<<<NB * DM / 128, 128, 0, stream>>>(x, pa, pb, (uint16_t*)d_out);
}
```

```cpp
#include <hip/hip_runtime.h>
#include <hip/hip_bf16.h>
#include <stdint.h>

typedef unsigned u32x4 __attribute__((ext_vector_type(4)));
typedef unsigned u32x2 __attribute__((ext_vector_type(2)));
typedef float f32x4 __attribute__((ext_vector_type(4)));
typedef float f32x2 __attribute__((ext_vector_type(2)));
typedef __bf16 bf16x2 __attribute__((ext_vector_type(2)));

constexpr int NB = 8, DM = 512, NH = 32, SK = 4096, DH = 128, FF = 1376, FS = 43;
constexpr int NW = 8, NT = NW * 64;
#define CFG_CK 4
constexpr int CK = CFG_CK;
constexpr int CL = CK / 4;
#define CFG_IB 32
constexpr int IB = CFG_IB;
constexpr int IL = IB / 4;
constexpr int NCH = (SK - 4 * IB) / (CK * NW);
#define CFG_STAG 12
constexpr int STAG = CFG_STAG;
constexpr int RW = DM / NW;
constexpr int QW = 4;
constexpr int QRW = DM / QW;
constexpr int QR = QRW / 4;
constexpr int FR = RW / 8;
constexpr int OR = DH / NW;
constexpr int DR = (FS + NW - 1) / NW;
constexpr float SCALE = 0.08838834764831845f;
constexpr float LOG2E = 1.4426950408889634f;
static_assert(STAG % 2 == 0 && STAG * (NW - 1) + 4 <= NCH && NCH >= 6 && NCH % 2 == 0 && RW == 64, "config");

constexpr int L_XN = 0;
constexpr int L_QP = L_XN + NW * 32;
constexpr int L_QB = L_QP + NW * 128;
constexpr int L_FP = L_QB + 64;
constexpr int L_REC = L_FP + NW * 128;
constexpr int L_RA = L_REC + NW * 132;
constexpr int L_RB = L_RA + NW * 512;
constexpr int L_END = L_RB + NW * 512;

__device__ __forceinline__ float bfr(float x) { return __bfloat162float(__float2bfloat16(x)); }
__device__ __forceinline__ float bf_lo(unsigned u) { return __builtin_bit_cast(float, u << 16); }
__device__ __forceinline__ float bf_hi(unsigned u) { return __builtin_bit_cast(float, u & 0xffff0000u); }
__device__ __forceinline__ float dot2(unsigned k, unsigned q, float acc) {
  return __builtin_amdgcn_fdot2_f32_bf16(__builtin_bit_cast(bf16x2, k), __builtin_bit_cast(bf16x2, q), acc, false);
}
__device__ __forceinline__ void axpy8(float (&acc)[8], const u32x4& row, unsigned slo, unsigned shi) {
#pragma unroll
  for (int j = 0; j < 4; ++j) { acc[2 * j] = dot2(row[j], slo, acc[2 * j]); acc[2 * j + 1] = dot2(row[j], shi, acc[2 * j + 1]); }
}
template <int CTRL> __device__ __forceinline__ float dpp(float x) {
  return __builtin_bit_cast(float, __builtin_amdgcn_mov_dpp(__builtin_bit_cast(int, x), CTRL, 0xf, 0xf, true));
}
constexpr int ROR1 = 0x121, ROR2 = 0x122, ROR4 = 0x124, ROR8 = 0x128;
__device__ __forceinline__ float row16_sum(float x) {
  x += dpp<ROR8>(x); x += dpp<ROR4>(x); x += dpp<ROR2>(x); x += dpp<ROR1>(x); return x;
}
__device__ __forceinline__ float xrow_sum(float x) {
  auto s = __builtin_amdgcn_permlane16_swap(__float_as_uint(x), __float_as_uint(x), false, false);
  x = __uint_as_float(s[0]) + __uint_as_float(s[1]);
  auto t = __builtin_amdgcn_permlane32_swap(__float_as_uint(x), __float_as_uint(x), false, false);
  return __uint_as_float(t[0]) + __uint_as_float(t[1]);
}
__device__ __forceinline__ float xrow_max(float x) {
  auto s = __builtin_amdgcn_permlane16_swap(__float_as_uint(x), __float_as_uint(x), false, false);
  x = fmaxf(__uint_as_float(s[0]), __uint_as_float(s[1]));
  auto t = __builtin_amdgcn_permlane32_swap(__float_as_uint(x), __float_as_uint(x), false, false);
  return fmaxf(__uint_as_float(t[0]), __uint_as_float(t[1]));
}
__device__ __forceinline__ u32x4 ldnt(const void* p) { return __builtin_nontemporal_load(reinterpret_cast<const u32x4*>(p)); }
__device__ __forceinline__ u32x4 ld16(const void* p) { return *reinterpret_cast<const u32x4*>(p); }
__device__ __forceinline__ u32x4 ldw(const void* p) { return ldnt(p); }
#define SCHED_FENCE() __builtin_amdgcn_sched_barrier(0)
#define LOOP_SYNC() __builtin_amdgcn_s_barrier()

__global__ void __launch_bounds__(NT) fused_decode_kernel(
    const uint16_t* __restrict__ x, const uint16_t* __restrict__ kc, const uint16_t* __restrict__ vc,
    const uint16_t* __restrict__ wq, const uint16_t* __restrict__ wo, const uint16_t* __restrict__ wg,
    const uint16_t* __restrict__ wu, const uint16_t* __restrict__ wd, float* __restrict__ pa,
    float* __restrict__ pb) {
  __shared__ __attribute__((aligned(16))) float lds[L_END];
  const int h = blockIdx.x & (NH - 1), b = blockIdx.x >> 5;
  const int tid = threadIdx.x, lane = tid & 63, w = __builtin_amdgcn_readfirstlane(tid >> 6);
  const int r = lane >> 4, c = lane & 15, r8 = lane >> 3, c8 = lane & 7;

  const char* kbase = reinterpret_cast<const char*>(kc) + (size_t)(b * NH + h) * (SK * DH * 2) + (size_t)(4 * IB + w * CK) * (DH * 2);
  const char* vbase = reinterpret_cast<const char*>(vc) + (size_t)(b * NH + h) * (SK * DH * 2) + (size_t)(4 * IB + w * CK) * (DH * 2);
  u32x4 KI[IL], VI[IL];
  const unsigned loff = lane * 16;
  constexpr size_t CSTRIDE = (size_t)NW * CK * DH * 2;
  u32x4 KA[CL], VA[CL], KB[CL], VB[CL];
#define ISSUE(KR, VR, ch) do { _Pragma("unroll") for (int i_ = 0; i_ < CL; ++i_) KR[i_] = ldnt(kbase + (size_t)(ch) * CSTRIDE + i_ * 1024 + loff); \
                               _Pragma("unroll") for (int i_ = 0; i_ < CL; ++i_) VR[i_] = ldnt(vbase + (size_t)(ch) * CSTRIDE + i_ * 1024 + loff); } while (0)
  uint16_t* xn16 = reinterpret_cast<uint16_t*>(&lds[L_XN]);
  if (w < QW) {
    const uint16_t* xb = x + b * DM;
    const u32x4 xall = ld16(xb + 8 * lane);
    const unsigned xm0 = xb[QRW * w + lane], xm1 = xb[QRW * w + 64 + lane];
    u32x4 wqv[QR];
    {
      const char* base = reinterpret_cast<const char*>(wq) + (size_t)(QRW * w) * (NH * DH * 2) + h * (DH * 2);
      const unsigned voff = (unsigned)(QR * r) * (NH * DH * 2) + 16 * c;
#pragma unroll
      for (int i = 0; i < QR; ++i) wqv[i] = ldw(base + (size_t)i * (NH * DH * 2) + voff);
    }
    SCHED_FENCE();
    __builtin_amdgcn_s_barrier();
    SCHED_FENCE();
    {
      float xs[8];
#pragma unroll
      for (int j = 0; j < 4; ++j) { xs[2 * j] = bf_lo(xall[j]); xs[2 * j + 1] = bf_hi(xall[j]); }
      float s = ((xs[0] + xs[1]) + (xs[2] + xs[3])) + ((xs[4] + xs[5]) + (xs[6] + xs[7]));
      s = xrow_sum(row16_sum(s));
      const float mean = s * (1.0f / DM);
      float v = 0.f;
#pragma unroll
      for (int j = 0; j < 8; ++j) { const float d = xs[j] - mean; v = fmaf(d, d, v); }
      v = xrow_sum(row16_sum(v));
      const float var_b = bfr(v * (1.0f / DM));
      const float mean_b = bfr(mean);
      const float den = bfr(sqrtf(bfr(var_b + 1e-5f)));
      const float xn0 = bfr(bfr(bf_lo(xm0) - mean_b) / den);
      const float xn1 = bfr(bfr(bf_lo(xm1) - mean_b) / den);
      xn16[QRW * w + lane] = (uint16_t)(__builtin_bit_cast(unsigned, xn0) >> 16);
      xn16[QRW * w + 64 + lane] = (uint16_t)(__builtin_bit_cast(unsigned, xn1) >> 16);
    }
    u32x4 xa[QR / 8];
#pragma unroll
    for (int i = 0; i < QR / 8; ++i) xa[i] = *reinterpret_cast<const u32x4*>(xn16 + QRW * w + QR * r + 8 * i);
    float acc[8];
#pragma unroll
    for (int j = 0; j < 8; ++j) acc[j] = 0.f;
#pragma unroll
    for (int i = 0; i < QR; ++i) {
      const unsigned p = xa[i >> 3][(i >> 1) & 3];
      axpy8(acc, wqv[i], (i & 1) ? (p >> 16) : (p & 0xffffu), (i & 1) ? (p & 0xffff0000u) : (p << 16));
    }
#pragma unroll
    for (int j = 0; j < 8; ++j) acc[j] = xrow_sum(acc[j]);
    if (r == 0) {
      *reinterpret_cast<f32x4*>(&lds[L_QP + w * 128 + 8 * c]) = f32x4{acc[0], acc[1], acc[2], acc[3]};
      *reinterpret_cast<f32x4*>(&lds[L_QP + w * 128 + 8 * c + 4]) = f32x4{acc[4], acc[5], acc[6], acc[7]};
    }
  } else {
    __builtin_amdgcn_s_barrier();
    SCHED_FENCE();
    const char* ki = reinterpret_cast<const char*>(kc) + (size_t)(b * NH + h) * (SK * DH * 2) + (size_t)((w - QW) * IB) * (DH * 2);
    const char* vi = reinterpret_cast<const char*>(vc) + (size_t)(b * NH + h) * (SK * DH * 2) + (size_t)((w - QW) * IB) * (DH * 2);
#pragma unroll
    for (int i = 0; i < IL; ++i) KI[i] = ldnt(ki + i * 1024 + loff);
#pragma unroll
    for (int i = 0; i < IL; ++i) VI[i] = ldnt(vi + i * 1024 + loff);
  }
  SCHED_FENCE();
  __syncthreads();
  if (tid < 128) {
    float s = 0.f;
#pragma unroll
    for (int i = 0; i < QW; ++i) s += lds[L_QP + i * 128 + tid];
    reinterpret_cast<uint16_t*>(&lds[L_QB])[tid] = (uint16_t)(__builtin_bit_cast(unsigned, bfr(s)) >> 16);
  }
  SCHED_FENCE();
  __syncthreads();
  ISSUE(KA, VA, 0);
  ISSUE(KB, VB, 1);
  SCHED_FENCE();
  const u32x4 q = *reinterpret_cast<const u32x4*>(&lds[L_QB + 4 * c]);

  float m = -INFINITY, l = 0.f, acc[8];
#pragma unroll
  for (int j = 0; j < 8; ++j) acc[j] = 0.f;
#define CONSUME(KR, VR) CONSUMEN(KR, VR, CL)
#define CONSUMEN(KR, VR, CL) do { \
    float s_[CL]; float mn_ = m; \
    _Pragma("unroll") for (int i_ = 0; i_ < CL; ++i_) { \
      float d_ = dot2(KR[i_][0], q[0], 0.f); d_ = dot2(KR[i_][1], q[1], d_); d_ = dot2(KR[i_][2], q[2], d_); d_ = dot2(KR[i_][3], q[3], d_); \
      d_ = row16_sum(d_); s_[i_] = bfr(bfr(d_) * SCALE) * LOG2E; mn_ = fmaxf(mn_, s_[i_]); } \
    const float al_ = __builtin_amdgcn_exp2f(m - mn_); m = mn_; \
    float ps_ = 0.f; \
    _Pragma("unroll") for (int i_ = 0; i_ < CL; ++i_) { s_[i_] = __builtin_amdgcn_exp2f(s_[i_] - mn_); ps_ += s_[i_]; } \
    l = fmaf(l, al_, ps_); \
    _Pragma("unroll") for (int j_ = 0; j_ < 8; ++j_) acc[j_] *= al_; \
    _Pragma("unroll") for (int i_ = 0; i_ < CL; ++i_) { \
      _Pragma("unroll") for (int j_ = 0; j_ < 4; ++j_) { \
        acc[2 * j_] = fmaf(s_[i_], bf_lo(VR[i_][j_]), acc[2 * j_]); \
        acc[2 * j_ + 1] = fmaf(s_[i_], bf_hi(VR[i_][j_]), acc[2 * j_ + 1]); } } \
    asm volatile("" : "+v"(acc[0]), "+v"(acc[1]), "+v"(acc[2]), "+v"(acc[3]), "+v"(acc[4]), "+v"(acc[5]), "+v"(acc[6]), "+v"(acc[7]), "+v"(l), "+v"(m)); \
  } while (0)
#define STEP2(it) do { \
    CONSUME(KA, VA); SCHED_FENCE(); ISSUE(KA, VA, (it) + 2); SCHED_FENCE(); \
    CONSUME(KB, VB); SCHED_FENCE(); ISSUE(KB, VB, (it) + 3); SCHED_FENCE(); } while (0)

  if (w >= QW) {
#pragma unroll
    for (int i = 0; i < IL; i += 4) { CONSUMEN((KI + i), (VI + i), 4); SCHED_FENCE(); }
  }
  const int it_w = STAG * w;
#pragma unroll 1
  for (int it = 0; it < it_w; it += 2) STEP2(it);
  const int c0 = (FS * h) >> 3;
  const int foff = FS * h - 8 * c0;
  u32x4 gv[FR], uv[FR];
  {
    const unsigned voff = (unsigned)(FR * r8) * (FF * 2) + 16 * min(c0 + c8, FF / 8 - 1);
    const char* gb = reinterpret_cast<const char*>(wg) + (size_t)(RW * w) * (FF * 2);
    const char* ub = reinterpret_cast<const char*>(wu) + (size_t)(RW * w) * (FF * 2);
#pragma unroll
    for (int i = 0; i < FR; ++i) { gv[i] = ldw(gb + (size_t)i * (FF * 2) + voff); uv[i] = ldw(ub + (size_t)i * (FF * 2) + voff); }
  }
  SCHED_FENCE();
  STEP2(it_w);
  {
    const u32x4 xa = *reinterpret_cast<const u32x4*>(xn16 + RW * w + FR * r8);
    float ag[8], au[8];
#pragma unroll
    for (int j = 0; j < 8; ++j) { ag[j] = 0.f; au[j] = 0.f; }
#pragma unroll
    for (int i = 0; i < FR; ++i) {
      const unsigned p = xa[i >> 1];
      const unsigned slo = (i & 1) ? (p >> 16) : (p & 0xffffu), shi = (i & 1) ? (p & 0xffff0000u) : (p << 16);
      axpy8(ag, gv[i], slo, shi);
      axpy8(au, uv[i], slo, shi);
    }
#pragma unroll
    for (int j = 0; j < 8; ++j) {
      ag[j] += dpp<ROR8>(ag[j]); ag[j] = xrow_sum(ag[j]);
      au[j] += dpp<ROR8>(au[j]); au[j] = xrow_sum(au[j]);
    }
    if (r8 == 0) {
      float* fp = &lds[L_FP + w * 128 + 8 * c8];
      *reinterpret_cast<f32x4*>(fp) = f32x4{ag[0], ag[1], ag[2], ag[3]};
      *reinterpret_cast<f32x4*>(fp + 4) = f32x4{ag[4], ag[5], ag[6], ag[7]};
      *reinterpret_cast<f32x4*>(fp + 64) = f32x4{au[0], au[1], au[2], au[3]};
      *reinterpret_cast<f32x4*>(fp + 68) = f32x4{au[4], au[5], au[6], au[7]};
    }
  }
  SCHED_FENCE();
  u32x4 wov[OR], wdv[DR];
  {
    const char* ob = reinterpret_cast<const char*>(wo) + (size_t)(h * DH + OR * w) * (DM * 2);
    const char* db = reinterpret_cast<const char*>(wd) + (size_t)(FS * h) * (DM * 2);
#pragma unroll
    for (int i = 0; i < OR; ++i) wov[i] = ldw(ob + i * (DM * 2) + loff);
#pragma unroll
    for (int i = 0; i < DR; ++i) wdv[i] = ldw(db + (size_t)min(w + NW * i, FS - 1) * (DM * 2) + loff);
  }
  SCHED_FENCE();
#pragma unroll 1
  for (int it = it_w + 2; it < NCH - 2; it += 2) STEP2(it);
  CONSUME(KA, VA);
  SCHED_FENCE();
  CONSUME(KB, VB);
#undef STEP2
#undef CONSUME
#undef CONSUMEN
#undef ISSUE

  {
    const float M = xrow_max(m);
    const float wt = __builtin_amdgcn_exp2f(m - M);
    l = xrow_sum(l * wt);
#pragma unroll
    for (int j = 0; j < 8; ++j) acc[j] = xrow_sum(acc[j] * wt);
    if (r == 0) {
      float* rec = &lds[L_REC + w * 132];
      *reinterpret_cast<f32x4*>(rec + 4 + 8 * c) = f32x4{acc[0], acc[1], acc[2], acc[3]};
      *reinterpret_cast<f32x4*>(rec + 8 + 8 * c) = f32x4{acc[4], acc[5], acc[6], acc[7]};
      if (c == 0) *reinterpret_cast<f32x2*>(rec) = f32x2{M, l};
    }
  }
  SCHED_FENCE();
  __syncthreads();
  SCHED_FENCE();
  {
    f32x2 ml[NW];
    float av[NW], g = 0.f, u = 0.f;
    const int jr = min(w + NW * (lane & 7), FS - 1);
#pragma unroll
    for (int i = 0; i < NW; ++i) {
      ml[i] = *reinterpret_cast<const f32x2*>(&lds[L_REC + i * 132]);
      av[i] = lds[L_REC + i * 132 + 4 + OR * w + c];
      g += lds[L_FP + i * 128 + foff + jr];
      u += lds[L_FP + i * 128 + 64 + foff + jr];
    }
    float M = ml[0][0];
#pragma unroll
    for (int i = 1; i < NW; ++i) M = fmaxf(M, ml[i][0]);
    float L = 0.f, o = 0.f;
#pragma unroll
    for (int i = 0; i < NW; ++i) {
      const float wt = __builtin_amdgcn_exp2f(ml[i][0] - M);
      L = fmaf(wt, ml[i][1], L);
      o = fmaf(wt, av[i], o);
    }
    const unsigned ob = __builtin_bit_cast(unsigned, bfr(o / L));
    g = bfr(g); u = bfr(u);
    g = g > 0.f ? g : 0.f;
    const unsigned hb = __builtin_bit_cast(unsigned, bfr(g * u));
    float a[8], bb[8];
#pragma unroll
    for (int j = 0; j < 8; ++j) { a[j] = 0.f; bb[j] = 0.f; }
#pragma unroll
    for (int i = 0; i < OR; ++i) {
      const unsigned s = __builtin_amdgcn_readlane(ob, i);
      axpy8(a, wov[i], s >> 16, s);
    }
#pragma unroll
    for (int t = 0; t < DR; ++t) {
      unsigned s = __builtin_amdgcn_readlane(hb, t);
      if (w + NW * t >= FS) s = 0u;
      axpy8(bb, wdv[t], s >> 16, s);
    }
    *reinterpret_cast<f32x4*>(&lds[L_RA + w * 512 + 8 * lane]) = f32x4{a[0], a[1], a[2], a[3]};
    *reinterpret_cast<f32x4*>(&lds[L_RA + w * 512 + 8 * lane + 4]) = f32x4{a[4], a[5], a[6], a[7]};
    *reinterpret_cast<f32x4*>(&lds[L_RB + w * 512 + 8 * lane]) = f32x4{bb[0], bb[1], bb[2], bb[3]};
    *reinterpret_cast<f32x4*>(&lds[L_RB + w * 512 + 8 * lane + 4]) = f32x4{bb[4], bb[5], bb[6], bb[7]};
  }
  __syncthreads();
  {
    float sa = 0.f, sb = 0.f;
#pragma unroll
    for (int i = 0; i < NW; ++i) { sa += lds[L_RA + i * 512 + tid]; sb += lds[L_RB + i * 512 + tid]; }
    pa[(size_t)(b * NH + h) * DM + tid] = sa;
    pb[(size_t)(b * NH + h) * DM + tid] = sb;
  }
}

__global__ void __launch_bounds__(128) reduce_kernel(const uint16_t* __restrict__ x, const float* __restrict__ pa,
                                                     const float* __restrict__ pb, uint16_t* __restrict__ out) {
  const int idx = blockIdx.x * 128 + threadIdx.x;
  const int b = idx >> 9, n = idx & 511;
  float va[NH], vb[NH];
#pragma unroll
  for (int i = 0; i < NH; ++i) { va[i] = pa[(size_t)(b * NH + i) * DM + n]; vb[i] = pb[(size_t)(b * NH + i) * DM + n]; }
  float sa = 0.f, sb = 0.f;
#pragma unroll
  for (int i = 0; i < NH; ++i) { sa += va[i]; sb += vb[i]; }
  const float xv = __builtin_bit_cast(float, (unsigned)x[idx] << 16);
  const float o = bfr(bfr(xv + bfr(sa)) + bfr(sb));
  out[idx] = (uint16_t)(__builtin_bit_cast(unsigned, o) >> 16);
}

extern "C" void kernel_launch(void* const* d_in, const int* in_sizes, int n_in, void* d_out, int out_size,
                              void* d_ws, size_t ws_size, hipStream_t stream) {
  const uint16_t* x = (const uint16_t*)d_in[0];
  const uint16_t* kc = (const uint16_t*)d_in[1];
  const uint16_t* vc = (const uint16_t*)d_in[2];
  const uint16_t* wq = (const uint16_t*)d_in[3];
  const uint16_t* wo = (const uint16_t*)d_in[4];
  const uint16_t* wg = (const uint16_t*)d_in[6];
  const uint16_t* wu = (const uint16_t*)d_in[7];
  const uint16_t* wd = (const uint16_t*)d_in[8];
  float* pa = (float*)d_ws;
  float* pb = pa + NB * NH * DM;
  fused_decode_kernel<<<NB * NH, NT, 0, stream>>>(x, kc, vc, wq, wo, wg, wu, wd, pa, pb);
  reduce_kernel<<<NB * DM / 128, 128, 0, stream>>>(x, pa, pb, (uint16_t*)d_out);
}
```

```cpp
#define CFG_VB 4
#define W_PLAIN 1
#include <hip/hip_runtime.h>
#include <hip/hip_bf16.h>
#include <stdint.h>

typedef unsigned u32x4 __attribute__((ext_vector_type(4)));
typedef unsigned u32x2 __attribute__((ext_vector_type(2)));
typedef float f32x4 __attribute__((ext_vector_type(4)));
typedef float f32x2 __attribute__((ext_vector_type(2)));
typedef __bf16 bf16x2 __attribute__((ext_vector_type(2)));

constexpr int NB = 8, DM = 512, NH = 32, SK = 4096, DH = 128, FF = 1376, FS = 43;
constexpr int NW = 8, NT = NW * 64;
#define CFG_CK 4
constexpr int CK = CFG_CK;
constexpr int CL = CK / 4;
#define CFG_IB 32
constexpr int IB = CFG_IB;
constexpr int IL = IB / 4;
constexpr int NCH = (SK - 4 * IB) / (CK * NW);
#define CFG_STAG (32 / CFG_CK)
constexpr int STAG = CFG_STAG;
constexpr int RW = DM / NW;
constexpr int QW = 4;
constexpr int QRW = DM / QW;
constexpr int QR = QRW / 4;
constexpr int FR = RW / 8;
constexpr int OR = DH / NW;
constexpr int DR = (FS + NW - 1) / NW;
constexpr float SCALE = 0.08838834764831845f;
constexpr float LOG2E = 1.4426950408889634f;
static_assert(STAG % 2 == 0 && STAG * (NW - 1) + 4 <= NCH && NCH >= 6 && NCH % 2 == 0 && RW == 64, "config");

constexpr int L_XN = 0;
constexpr int L_QP = L_XN + NW * 32;
constexpr int L_QB = L_QP + NW * 128;
constexpr int L_FP = L_QB + 64;
constexpr int L_REC = L_FP + NW * 128;
constexpr int L_RA = L_REC + NW * 132;
constexpr int L_RB = L_RA + NW * 512;
constexpr int L_END = L_RB + NW * 512;

__device__ __forceinline__ float bfr(float x) { return __bfloat162float(__float2bfloat16(x)); }
__device__ __forceinline__ float bf_lo(unsigned u) { return __builtin_bit_cast(float, u << 16); }
__device__ __forceinline__ float bf_hi(unsigned u) { return __builtin_bit_cast(float, u & 0xffff0000u); }
__device__ __forceinline__ float dot2(unsigned k, unsigned q, float acc) {
  return __builtin_amdgcn_fdot2_f32_bf16(__builtin_bit_cast(bf16x2, k), __builtin_bit_cast(bf16x2, q), acc, false);
}
__device__ __forceinline__ void axpy8(float (&acc)[8], const u32x4& row, unsigned slo, unsigned shi) {
#pragma unroll
  for (int j = 0; j < 4; ++j) { acc[2 * j] = dot2(row[j], slo, acc[2 * j]); acc[2 * j + 1] = dot2(row[j], shi, acc[2 * j + 1]); }
}
template <int CTRL> __device__ __forceinline__ float dpp(float x) {
  return __builtin_bit_cast(float, __builtin_amdgcn_mov_dpp(__builtin_bit_cast(int, x), CTRL, 0xf, 0xf, true));
}
constexpr int ROR1 = 0x121, ROR2 = 0x122, ROR4 = 0x124, ROR8 = 0x128;
__device__ __forceinline__ float row16_sum(float x) {
  x += dpp<ROR8>(x); x += dpp<ROR4>(x); x += dpp<ROR2>(x); x += dpp<ROR1>(x); return x;
}
__device__ __forceinline__ float xrow_sum(float x) {
  auto s = __builtin_amdgcn_permlane16_swap(__float_as_uint(x), __float_as_uint(x), false, false);
  x = __uint_as_float(s[0]) + __uint_as_float(s[1]);
  auto t = __builtin_amdgcn_permlane32_swap(__float_as_uint(x), __float_as_uint(x), false, false);
  return __uint_as_float(t[0]) + __uint_as_float(t[1]);
}
__device__ __forceinline__ float xrow_max(float x) {
  auto s = __builtin_amdgcn_permlane16_swap(__float_as_uint(x), __float_as_uint(x), false, false);
  x = fmaxf(__uint_as_float(s[0]), __uint_as_float(s[1]));
  auto t = __builtin_amdgcn_permlane32_swap(__float_as_uint(x), __float_as_uint(x), false, false);
  return fmaxf(__uint_as_float(t[0]), __uint_as_float(t[1]));
}
__device__ __forceinline__ u32x4 ldnt(const void* p) { return __builtin_nontemporal_load(reinterpret_cast<const u32x4*>(p)); }
__device__ __forceinline__ u32x4 ld16(const void* p) { return *reinterpret_cast<const u32x4*>(p); }
__device__ __forceinline__ u32x4 ldw(const void* p) { return ld16(p); }
#define SCHED_FENCE() __builtin_amdgcn_sched_barrier(0)
#define LOOP_SYNC() __builtin_amdgcn_s_barrier()

template <bool P> __device__ __forceinline__ u32x4 ldp(const void* p) { return P ? ld16(p) : ldnt(p); }
template <bool KP, bool VP> __device__ __forceinline__ void fused_body(float* lds,
    const uint16_t* __restrict__ x, const uint16_t* __restrict__ kc, const uint16_t* __restrict__ vc,
    const uint16_t* __restrict__ wq, const uint16_t* __restrict__ wo, const uint16_t* __restrict__ wg,
    const uint16_t* __restrict__ wu, const uint16_t* __restrict__ wd, float* __restrict__ pa,
    float* __restrict__ pb) {
  const int h = blockIdx.x & (NH - 1), b = blockIdx.x >> 5;
  const int tid = threadIdx.x, lane = tid & 63, w = __builtin_amdgcn_readfirstlane(tid >> 6);
  const int r = lane >> 4, c = lane & 15, r8 = lane >> 3, c8 = lane & 7;

  const char* kbase = reinterpret_cast<const char*>(kc) + (size_t)(b * NH + h) * (SK * DH * 2) + (size_t)(4 * IB + w * CK) * (DH * 2);
  const char* vbase = reinterpret_cast<const char*>(vc) + (size_t)(b * NH + h) * (SK * DH * 2) + (size_t)(4 * IB + w * CK) * (DH * 2);
  u32x4 KI[IL], VI[IL];
  const unsigned loff = lane * 16;
  constexpr size_t CSTRIDE = (size_t)NW * CK * DH * 2;
  u32x4 KA[CL], VA[CL], KB[CL], VB[CL];
#define ISSUE(KR, VR, ch) do { _Pragma("unroll") for (int i_ = 0; i_ < CL; ++i_) KR[i_] = ldp<KP>(kbase + (size_t)(ch) * CSTRIDE + i_ * 1024 + loff); \
                               _Pragma("unroll") for (int i_ = 0; i_ < CL; ++i_) VR[i_] = ldp<VP>(vbase + (size_t)(ch) * CSTRIDE + i_ * 1024 + loff); } while (0)
  uint16_t* xn16 = reinterpret_cast<uint16_t*>(&lds[L_XN]);
  if (w < QW) {
    const uint16_t* xb = x + b * DM;
    const u32x4 xall = ld16(xb + 8 * lane);
    const unsigned xm0 = xb[QRW * w + lane], xm1 = xb[QRW * w + 64 + lane];
    u32x4 wqv[QR];
    {
      const char* base = reinterpret_cast<const char*>(wq) + (size_t)(QRW * w) * (NH * DH * 2) + h * (DH * 2);
      const unsigned voff = (unsigned)(QR * r) * (NH * DH * 2) + 16 * c;
#pragma unroll
      for (int i = 0; i < QR; ++i) wqv[i] = ldw(base + (size_t)i * (NH * DH * 2) + voff);
    }
    SCHED_FENCE();
    __builtin_amdgcn_s_barrier();
    SCHED_FENCE();
    {
      float xs[8];
#pragma unroll
      for (int j = 0; j < 4; ++j) { xs[2 * j] = bf_lo(xall[j]); xs[2 * j + 1] = bf_hi(xall[j]); }
      float s = ((xs[0] + xs[1]) + (xs[2] + xs[3])) + ((xs[4] + xs[5]) + (xs[6] + xs[7]));
      s = xrow_sum(row16_sum(s));
      const float mean = s * (1.0f / DM);
      float v = 0.f;
#pragma unroll
      for (int j = 0; j < 8; ++j) { const float d = xs[j] - mean; v = fmaf(d, d, v); }
      v = xrow_sum(row16_sum(v));
      const float var_b = bfr(v * (1.0f / DM));
      const float mean_b = bfr(mean);
      const float den = bfr(sqrtf(bfr(var_b + 1e-5f)));
      const float xn0 = bfr(bfr(bf_lo(xm0) - mean_b) / den);
      const float xn1 = bfr(bfr(bf_lo(xm1) - mean_b) / den);
      xn16[QRW * w + lane] = (uint16_t)(__builtin_bit_cast(unsigned, xn0) >> 16);
      xn16[QRW * w + 64 + lane] = (uint16_t)(__builtin_bit_cast(unsigned, xn1) >> 16);
    }
    u32x4 xa[QR / 8];
#pragma unroll
    for (int i = 0; i < QR / 8; ++i) xa[i] = *reinterpret_cast<const u32x4*>(xn16 + QRW * w + QR * r + 8 * i);
    float acc[8];
#pragma unroll
    for (int j = 0; j < 8; ++j) acc[j] = 0.f;
#pragma unroll
    for (int i = 0; i < QR; ++i) {
      const unsigned p = xa[i >> 3][(i >> 1) & 3];
      axpy8(acc, wqv[i], (i & 1) ? (p >> 16) : (p & 0xffffu), (i & 1) ? (p & 0xffff0000u) : (p << 16));
    }
#pragma unroll
    for (int j = 0; j < 8; ++j) acc[j] = xrow_sum(acc[j]);
    if (r == 0) {
      *reinterpret_cast<f32x4*>(&lds[L_QP + w * 128 + 8 * c]) = f32x4{acc[0], acc[1], acc[2], acc[3]};
      *reinterpret_cast<f32x4*>(&lds[L_QP + w * 128 + 8 * c + 4]) = f32x4{acc[4], acc[5], acc[6], acc[7]};
    }
  } else {
    __builtin_amdgcn_s_barrier();
    SCHED_FENCE();
    const char* ki = reinterpret_cast<const char*>(kc) + (size_t)(b * NH + h) * (SK * DH * 2) + (size_t)((w - QW) * IB) * (DH * 2);
    const char* vi = reinterpret_cast<const char*>(vc) + (size_t)(b * NH + h) * (SK * DH * 2) + (size_t)((w - QW) * IB) * (DH * 2);
#pragma unroll
    for (int i = 0; i < IL; ++i) KI[i] = ldp<KP>(ki + i * 1024 + loff);
#pragma unroll
    for (int i = 0; i < IL; ++i) VI[i] = ldp<VP>(vi + i * 1024 + loff);
  }
  SCHED_FENCE();
  __syncthreads();
  if (tid < 128) {
    float s = 0.f;
#pragma unroll
    for (int i = 0; i < QW; ++i) s += lds[L_QP + i * 128 + tid];
    reinterpret_cast<uint16_t*>(&lds[L_QB])[tid] = (uint16_t)(__builtin_bit_cast(unsigned, bfr(s)) >> 16);
  }
  SCHED_FENCE();
  __syncthreads();
  ISSUE(KA, VA, 0);
  ISSUE(KB, VB, 1);
  SCHED_FENCE();
  const u32x4 q = *reinterpret_cast<const u32x4*>(&lds[L_QB + 4 * c]);

  float m = -INFINITY, l = 0.f, acc[8];
#pragma unroll
  for (int j = 0; j < 8; ++j) acc[j] = 0.f;
#define CONSUME(KR, VR) CONSUMEN(KR, VR, CL)
#define CONSUMEN(KR, VR, CL) do { \
    float s_[CL]; float mn_ = m; \
    _Pragma("unroll") for (int i_ = 0; i_ < CL; ++i_) { \
      float d_ = dot2(KR[i_][0], q[0], 0.f); d_ = dot2(KR[i_][1], q[1], d_); d_ = dot2(KR[i_][2], q[2], d_); d_ = dot2(KR[i_][3], q[3], d_); \
      d_ = row16_sum(d_); s_[i_] = bfr(bfr(d_) * SCALE) * LOG2E; mn_ = fmaxf(mn_, s_[i_]); } \
    const float al_ = __builtin_amdgcn_exp2f(m - mn_); m = mn_; \
    float ps_ = 0.f; \
    _Pragma("unroll") for (int i_ = 0; i_ < CL; ++i_) { s_[i_] = __builtin_amdgcn_exp2f(s_[i_] - mn_); ps_ += s_[i_]; } \
    l = fmaf(l, al_, ps_); \
    _Pragma("unroll") for (int j_ = 0; j_ < 8; ++j_) acc[j_] *= al_; \
    _Pragma("unroll") for (int i_ = 0; i_ < CL; ++i_) { \
      _Pragma("unroll") for (int j_ = 0; j_ < 4; ++j_) { \
        acc[2 * j_] = fmaf(s_[i_], bf_lo(VR[i_][j_]), acc[2 * j_]); \
        acc[2 * j_ + 1] = fmaf(s_[i_], bf_hi(VR[i_][j_]), acc[2 * j_ + 1]); } } \
    asm volatile("" : "+v"(acc[0]), "+v"(acc[1]), "+v"(acc[2]), "+v"(acc[3]), "+v"(acc[4]), "+v"(acc[5]), "+v"(acc[6]), "+v"(acc[7]), "+v"(l), "+v"(m)); \
  } while (0)
#define STEP2(it) do { \
    CONSUME(KA, VA); SCHED_FENCE(); ISSUE(KA, VA, (it) + 2); SCHED_FENCE(); \
    CONSUME(KB, VB); SCHED_FENCE(); ISSUE(KB, VB, (it) + 3); SCHED_FENCE(); } while (0)

  if (w >= QW) {
#pragma unroll
    for (int i = 0; i < IL; i += 4) { CONSUMEN((KI + i), (VI + i), 4); SCHED_FENCE(); }
  }
  const int it_w = STAG * w;
#pragma unroll 1
  for (int it = 0; it < it_w; it += 2) STEP2(it);
  const int c0 = (FS * h) >> 3;
  const int foff = FS * h - 8 * c0;
  u32x4 gv[FR], uv[FR];
  {
    const unsigned voff = (unsigned)(FR * r8) * (FF * 2) + 16 * min(c0 + c8, FF / 8 - 1);
    const char* gb = reinterpret_cast<const char*>(wg) + (size_t)(RW * w) * (FF * 2);
    const char* ub = reinterpret_cast<const char*>(wu) + (size_t)(RW * w) * (FF * 2);
#pragma unroll
    for (int i = 0; i < FR; ++i) { gv[i] = ldw(gb + (size_t)i * (FF * 2) + voff); uv[i] = ldw(ub + (size_t)i * (FF * 2) + voff); }
  }
  SCHED_FENCE();
  STEP2(it_w);
  {
    const u32x4 xa = *reinterpret_cast<const u32x4*>(xn16 + RW * w + FR * r8);
    float ag[8], au[8];
#pragma unroll
    for (int j = 0; j < 8; ++j) { ag[j] = 0.f; au[j] = 0.f; }
#pragma unroll
    for (int i = 0; i < FR; ++i) {
      const unsigned p = xa[i >> 1];
      const unsigned slo = (i & 1) ? (p >> 16) : (p & 0xffffu), shi = (i & 1) ? (p & 0xffff0000u) : (p << 16);
      axpy8(ag, gv[i], slo, shi);
      axpy8(au, uv[i], slo, shi);
    }
#pragma unroll
    for (int j = 0; j < 8; ++j) {
      ag[j] += dpp<ROR8>(ag[j]); ag[j] = xrow_sum(ag[j]);
      au[j] += dpp<ROR8>(au[j]); au[j] = xrow_sum(au[j]);
    }
    if (r8 == 0) {
      float* fp = &lds[L_FP + w * 128 + 8 * c8];
      *reinterpret_cast<f32x4*>(fp) = f32x4{ag[0], ag[1], ag[2], ag[3]};
      *reinterpret_cast<f32x4*>(fp + 4) = f32x4{ag[4], ag[5], ag[6], ag[7]};
      *reinterpret_cast<f32x4*>(fp + 64) = f32x4{au[0], au[1], au[2], au[3]};
      *reinterpret_cast<f32x4*>(fp + 68) = f32x4{au[4], au[5], au[6], au[7]};
    }
  }
  SCHED_FENCE();
  u32x4 wov[OR], wdv[DR];
  {
    const char* ob = reinterpret_cast<const char*>(wo) + (size_t)(h * DH + OR * w) * (DM * 2);
    const char* db = reinterpret_cast<const char*>(wd) + (size_t)(FS * h) * (DM * 2);
#pragma unroll
    for (int i = 0; i < OR; ++i) wov[i] = ldw(ob + i * (DM * 2) + loff);
#pragma unroll
    for (int i = 0; i < DR; ++i) wdv[i] = ldw(db + (size_t)min(w + NW * i, FS - 1) * (DM * 2) + loff);
  }
  SCHED_FENCE();
#pragma unroll 1
  for (int it = it_w + 2; it < NCH - 2; it += 2) STEP2(it);
  CONSUME(KA, VA);
  SCHED_FENCE();
  CONSUME(KB, VB);
#undef STEP2
#undef CONSUME
#undef CONSUMEN
#undef ISSUE

  {
    const float M = xrow_max(m);
    const float wt = __builtin_amdgcn_exp2f(m - M);
    l = xrow_sum(l * wt);
#pragma unroll
    for (int j = 0; j < 8; ++j) acc[j] = xrow_sum(acc[j] * wt);
    if (r == 0) {
      float* rec = &lds[L_REC + w * 132];
      *reinterpret_cast<f32x4*>(rec + 4 + 8 * c) = f32x4{acc[0], acc[1], acc[2], acc[3]};
      *reinterpret_cast<f32x4*>(rec + 8 + 8 * c) = f32x4{acc[4], acc[5], acc[6], acc[7]};
      if (c == 0) *reinterpret_cast<f32x2*>(rec) = f32x2{M, l};
    }
  }
  SCHED_FENCE();
  __syncthreads();
  SCHED_FENCE();
  {
    f32x2 ml[NW];
    float av[NW], g = 0.f, u = 0.f;
    const int jr = min(w + NW * (lane & 7), FS - 1);
#pragma unroll
    for (int i = 0; i < NW; ++i) {
      ml[i] = *reinterpret_cast<const f32x2*>(&lds[L_REC + i * 132]);
      av[i] = lds[L_REC + i * 132 + 4 + OR * w + c];
      g += lds[L_FP + i * 128 + foff + jr];
      u += lds[L_FP + i * 128 + 64 + foff + jr];
    }
    float M = ml[0][0];
#pragma unroll
    for (int i = 1; i < NW; ++i) M = fmaxf(M, ml[i][0]);
    float L = 0.f, o = 0.f;
#pragma unroll
    for (int i = 0; i < NW; ++i) {
      const float wt = __builtin_amdgcn_exp2f(ml[i][0] - M);
      L = fmaf(wt, ml[i][1], L);
      o = fmaf(wt, av[i], o);
    }
    const unsigned ob = __builtin_bit_cast(unsigned, bfr(o / L));
    g = bfr(g); u = bfr(u);
    g = g > 0.f ? g : 0.f;
    const unsigned hb = __builtin_bit_cast(unsigned, bfr(g * u));
    float a[8], bb[8];
#pragma unroll
    for (int j = 0; j < 8; ++j) { a[j] = 0.f; bb[j] = 0.f; }
#pragma unroll
    for (int i = 0; i < OR; ++i) {
      const unsigned s = __builtin_amdgcn_readlane(ob, i);
      axpy8(a, wov[i], s >> 16, s);
    }
#pragma unroll
    for (int t = 0; t < DR; ++t) {
      unsigned s = __builtin_amdgcn_readlane(hb, t);
      if (w + NW * t >= FS) s = 0u;
      axpy8(bb, wdv[t], s >> 16, s);
    }
    *reinterpret_cast<f32x4*>(&lds[L_RA + w * 512 + 8 * lane]) = f32x4{a[0], a[1], a[2], a[3]};
    *reinterpret_cast<f32x4*>(&lds[L_RA + w * 512 + 8 * lane + 4]) = f32x4{a[4], a[5], a[6], a[7]};
    *reinterpret_cast<f32x4*>(&lds[L_RB + w * 512 + 8 * lane]) = f32x4{bb[0], bb[1], bb[2], bb[3]};
    *reinterpret_cast<f32x4*>(&lds[L_RB + w * 512 + 8 * lane + 4]) = f32x4{bb[4], bb[5], bb[6], bb[7]};
  }
  __syncthreads();
  {
    float sa = 0.f, sb = 0.f;
#pragma unroll
    for (int i = 0; i < NW; ++i) { sa += lds[L_RA + i * 512 + tid]; sb += lds[L_RB + i * 512 + tid]; }
    pa[(size_t)(b * NH + h) * DM + tid] = sa;
    pb[(size_t)(b * NH + h) * DM + tid] = sb;
  }
}

#define CFG_KB 8
__global__ void __launch_bounds__(NT) fused_decode_kernel(
    const uint16_t* __restrict__ x, const uint16_t* __restrict__ kc, const uint16_t* __restrict__ vc,
    const uint16_t* __restrict__ wq, const uint16_t* __restrict__ wo, const uint16_t* __restrict__ wg,
    const uint16_t* __restrict__ wu, const uint16_t* __restrict__ wd, float* __restrict__ pa,
    float* __restrict__ pb) {
  __shared__ __attribute__((aligned(16))) float lds[L_END];
  const int b = blockIdx.x >> 5;
  if (b >= CFG_KB) fused_body<true, false>(lds, x, kc, vc, wq, wo, wg, wu, wd, pa, pb);
  else if (b >= CFG_VB) fused_body<false, true>(lds, x, kc, vc, wq, wo, wg, wu, wd, pa, pb);
  else fused_body<false, false>(lds, x, kc, vc, wq, wo, wg, wu, wd, pa, pb);
}

__global__ void __launch_bounds__(128) reduce_kernel(const uint16_t* __restrict__ x, const float* __restrict__ pa,
                                                     const float* __restrict__ pb, uint16_t* __restrict__ out) {
  const int idx = blockIdx.x * 128 + threadIdx.x;
  const int b = idx >> 9, n = idx & 511;
  float va[NH], vb[NH];
#pragma unroll
  for (int i = 0; i < NH; ++i) { va[i] = pa[(size_t)(b * NH + i) * DM + n]; vb[i] = pb[(size_t)(b * NH + i) * DM + n]; }
  float sa = 0.f, sb = 0.f;
#pragma unroll
  for (int i = 0; i < NH; ++i) { sa += va[i]; sb += vb[i]; }
  const float xv = __builtin_bit_cast(float, (unsigned)x[idx] << 16);
  const float o = bfr(bfr(xv + bfr(sa)) + bfr(sb));
  out[idx] = (uint16_t)(__builtin_bit_cast(unsigned, o) >> 16);
}

extern "C" void kernel_launch(void* const* d_in, const int* in_sizes, int n_in, void* d_out, int out_size,
                              void* d_ws, size_t ws_size, hipStream_t stream) {
  const uint16_t* x = (const uint16_t*)d_in[0];
  const uint16_t* kc = (const uint16_t*)d_in[1];
  const uint16_t* vc = (const uint16_t*)d_in[2];
  const uint16_t* wq = (const uint16_t*)d_in[3];
  const uint16_t* wo = (const uint16_t*)d_in[4];
  const uint16_t* wg = (const uint16_t*)d_in[6];
  const uint16_t* wu = (const uint16_t*)d_in[7];
  const uint16_t* wd = (const uint16_t*)d_in[8];
  float* pa = (float*)d_ws;
  float* pb = pa + NB * NH * DM;
  fused_decode_kernel<<<NB * NH, NT, 0, stream>>>(x, kc, vc, wq, wo, wg, wu, wd, pa, pb);
  reduce_kernel<<<NB * DM / 128, 128, 0, stream>>>(x, pa, pb, (uint16_t*)d_out);
}
```

```cpp
#define CFG_IB 64
#define W_PLAIN 1
#include <hip/hip_runtime.h>
#include <hip/hip_bf16.h>
#include <stdint.h>

typedef unsigned u32x4 __attribute__((ext_vector_type(4)));
typedef unsigned u32x2 __attribute__((ext_vector_type(2)));
typedef float f32x4 __attribute__((ext_vector_type(4)));
typedef float f32x2 __attribute__((ext_vector_type(2)));
typedef __bf16 bf16x2 __attribute__((ext_vector_type(2)));

constexpr int NB = 8, DM = 512, NH = 32, SK = 4096, DH = 128, FF = 1376, FS = 43;
constexpr int NW = 8, NT = NW * 64;
#define CFG_CK 4
constexpr int CK = CFG_CK;
constexpr int CL = CK / 4;
constexpr int IB = CFG_IB;
constexpr int IL = IB / 4;
constexpr int NCH = (SK - 4 * IB) / (CK * NW);
#define CFG_STAG (32 / CFG_CK)
constexpr int STAG = CFG_STAG;
constexpr int RW = DM / NW;
constexpr int QW = 4;
constexpr int QRW = DM / QW;
constexpr int QR = QRW / 4;
constexpr int FR = RW / 8;
constexpr int OR = DH / NW;
constexpr int DR = (FS + NW - 1) / NW;
constexpr float SCALE = 0.08838834764831845f;
constexpr float LOG2E = 1.4426950408889634f;
static_assert(STAG % 2 == 0 && STAG * (NW - 1) + 4 <= NCH && NCH >= 6 && NCH % 2 == 0 && RW == 64, "config");

constexpr int L_XN = 0;
constexpr int L_QP = L_XN + NW * 32;
constexpr int L_QB = L_QP + NW * 128;
constexpr int L_FP = L_QB + 64;
constexpr int L_REC = L_FP + NW * 128;
constexpr int L_RA = L_REC + NW * 132;
constexpr int L_RB = L_RA + NW * 512;
constexpr int L_END = L_RB + NW * 512;

__device__ __forceinline__ float bfr(float x) { return __bfloat162float(__float2bfloat16(x)); }
__device__ __forceinline__ float bf_lo(unsigned u) { return __builtin_bit_cast(float, u << 16); }
__device__ __forceinline__ float bf_hi(unsigned u) { return __builtin_bit_cast(float, u & 0xffff0000u); }
__device__ __forceinline__ float dot2(unsigned k, unsigned q, float acc) {
  return __builtin_amdgcn_fdot2_f32_bf16(__builtin_bit_cast(bf16x2, k), __builtin_bit_cast(bf16x2, q), acc, false);
}
__device__ __forceinline__ void axpy8(float (&acc)[8], const u32x4& row, unsigned slo, unsigned shi) {
#pragma unroll
  for (int j = 0; j < 4; ++j) { acc[2 * j] = dot2(row[j], slo, acc[2 * j]); acc[2 * j + 1] = dot2(row[j], shi, acc[2 * j + 1]); }
}
template <int CTRL> __device__ __forceinline__ float dpp(float x) {
  return __builtin_bit_cast(float, __builtin_amdgcn_mov_dpp(__builtin_bit_cast(int, x), CTRL, 0xf, 0xf, true));
}
constexpr int ROR1 = 0x121, ROR2 = 0x122, ROR4 = 0x124, ROR8 = 0x128;
__device__ __forceinline__ float row16_sum(float x) {
  x += dpp<ROR8>(x); x += dpp<ROR4>(x); x += dpp<ROR2>(x); x += dpp<ROR1>(x); return x;
}
__device__ __forceinline__ float xrow_sum(float x) {
  auto s = __builtin_amdgcn_permlane16_swap(__float_as_uint(x), __float_as_uint(x), false, false);
  x = __uint_as_float(s[0]) + __uint_as_float(s[1]);
  auto t = __builtin_amdgcn_permlane32_swap(__float_as_uint(x), __float_as_uint(x), false, false);
  return __uint_as_float(t[0]) + __uint_as_float(t[1]);
}
__device__ __forceinline__ float xrow_max(float x) {
  auto s = __builtin_amdgcn_permlane16_swap(__float_as_uint(x), __float_as_uint(x), false, false);
  x = fmaxf(__uint_as_float(s[0]), __uint_as_float(s[1]));
  auto t = __builtin_amdgcn_permlane32_swap(__float_as_uint(x), __float_as_uint(x), false, false);
  return fmaxf(__uint_as_float(t[0]), __uint_as_float(t[1]));
}
__device__ __forceinline__ u32x4 ldnt(const void* p) { return __builtin_nontemporal_load(reinterpret_cast<const u32x4*>(p)); }
__device__ __forceinline__ u32x4 ld16(const void* p) { return *reinterpret_cast<const u32x4*>(p); }
__device__ __forceinline__ u32x4 ldw(const void* p) { return ld16(p); }
#define SCHED_FENCE() __builtin_amdgcn_sched_barrier(0)
#define LOOP_SYNC() __builtin_amdgcn_s_barrier()

__global__ void __launch_bounds__(NT) fused_decode_kernel(
    const uint16_t* __restrict__ x, const uint16_t* __restrict__ kc, const uint16_t* __restrict__ vc,
    const uint16_t* __restrict__ wq, const uint16_t* __restrict__ wo, const uint16_t* __restrict__ wg,
    const uint16_t* __restrict__ wu, const uint16_t* __restrict__ wd, float* __restrict__ pa,
    float* __restrict__ pb) {
  __shared__ __attribute__((aligned(16))) float lds[L_END];
  const int h = blockIdx.x & (NH - 1), b = blockIdx.x >> 5;
  const int tid = threadIdx.x, lane = tid & 63, w = __builtin_amdgcn_readfirstlane(tid >> 6);
  const int r = lane >> 4, c = lane & 15, r8 = lane >> 3, c8 = lane & 7;

  const char* kbase = reinterpret_cast<const char*>(kc) + (size_t)(b * NH + h) * (SK * DH * 2) + (size_t)(4 * IB + w * CK) * (DH * 2);
  const char* vbase = reinterpret_cast<const char*>(vc) + (size_t)(b * NH + h) * (SK * DH * 2) + (size_t)(4 * IB + w * CK) * (DH * 2);
  u32x4 KI[IL], VI[IL];
  const unsigned loff = lane * 16;
  constexpr size_t CSTRIDE = (size_t)NW * CK * DH * 2;
  u32x4 KA[CL], VA[CL], KB[CL], VB[CL];
#define ISSUE(KR, VR, ch) do { _Pragma("unroll") for (int i_ = 0; i_ < CL; ++i_) KR[i_] = ldnt(kbase + (size_t)(ch) * CSTRIDE + i_ * 1024 + loff); \
                               _Pragma("unroll") for (int i_ = 0; i_ < CL; ++i_) VR[i_] = ldnt(vbase + (size_t)(ch) * CSTRIDE + i_ * 1024 + loff); } while (0)
  uint16_t* xn16 = reinterpret_cast<uint16_t*>(&lds[L_XN]);
  if (w < QW) {
    const uint16_t* xb = x + b * DM;
    const u32x4 xall = ld16(xb + 8 * lane);
    const unsigned xm0 = xb[QRW * w + lane], xm1 = xb[QRW * w + 64 + lane];
    u32x4 wqv[QR];
    {
      const char* base = reinterpret_cast<const char*>(wq) + (size_t)(QRW * w) * (NH * DH * 2) + h * (DH * 2);
      const unsigned voff = (unsigned)(QR * r) * (NH * DH * 2) + 16 * c;
#pragma unroll
      for (int i = 0; i < QR; ++i) wqv[i] = ldw(base + (size_t)i * (NH * DH * 2) + voff);
    }
    SCHED_FENCE();
    __builtin_amdgcn_s_barrier();
    SCHED_FENCE();
    {
      float xs[8];
#pragma unroll
      for (int j = 0; j < 4; ++j) { xs[2 * j] = bf_lo(xall[j]); xs[2 * j + 1] = bf_hi(xall[j]); }
      float s = ((xs[0] + xs[1]) + (xs[2] + xs[3])) + ((xs[4] + xs[5]) + (xs[6] + xs[7]));
      s = xrow_sum(row16_sum(s));
      const float mean = s * (1.0f / DM);
      float v = 0.f;
#pragma unroll
      for (int j = 0; j < 8; ++j) { const float d = xs[j] - mean; v = fmaf(d, d, v); }
      v = xrow_sum(row16_sum(v));
      const float var_b = bfr(v * (1.0f / DM));
      const float mean_b = bfr(mean);
      const float den = bfr(sqrtf(bfr(var_b + 1e-5f)));
      const float xn0 = bfr(bfr(bf_lo(xm0) - mean_b) / den);
      const float xn1 = bfr(bfr(bf_lo(xm1) - mean_b) / den);
      xn16[QRW * w + lane] = (uint16_t)(__builtin_bit_cast(unsigned, xn0) >> 16);
      xn16[QRW * w + 64 + lane] = (uint16_t)(__builtin_bit_cast(unsigned, xn1) >> 16);
    }
    u32x4 xa[QR / 8];
#pragma unroll
    for (int i = 0; i < QR / 8; ++i) xa[i] = *reinterpret_cast<const u32x4*>(xn16 + QRW * w + QR * r + 8 * i);
    float acc[8];
#pragma unroll
    for (int j = 0; j < 8; ++j) acc[j] = 0.f;
#pragma unroll
    for (int i = 0; i < QR; ++i) {
      const unsigned p = xa[i >> 3][(i >> 1) & 3];
      axpy8(acc, wqv[i], (i & 1) ? (p >> 16) : (p & 0xffffu), (i & 1) ? (p & 0xffff0000u) : (p << 16));
    }
#pragma unroll
    for (int j = 0; j < 8; ++j) acc[j] = xrow_sum(acc[j]);
    if (r == 0) {
      *reinterpret_cast<f32x4*>(&lds[L_QP + w * 128 + 8 * c]) = f32x4{acc[0], acc[1], acc[2], acc[3]};
      *reinterpret_cast<f32x4*>(&lds[L_QP + w * 128 + 8 * c + 4]) = f32x4{acc[4], acc[5], acc[6], acc[7]};
    }
  } else {
    __builtin_amdgcn_s_barrier();
    SCHED_FENCE();
    const char* ki = reinterpret_cast<const char*>(kc) + (size_t)(b * NH + h) * (SK * DH * 2) + (size_t)((w - QW) * IB) * (DH * 2);
    const char* vi = reinterpret_cast<const char*>(vc) + (size_t)(b * NH + h) * (SK * DH * 2) + (size_t)((w - QW) * IB) * (DH * 2);
#pragma unroll
    for (int i = 0; i < IL; ++i) KI[i] = ldnt(ki + i * 1024 + loff);
#pragma unroll
    for (int i = 0; i < IL; ++i) VI[i] = ldnt(vi + i * 1024 + loff);
  }
  SCHED_FENCE();
  __syncthreads();
  if (tid < 128) {
    float s = 0.f;
#pragma unroll
    for (int i = 0; i < QW; ++i) s += lds[L_QP + i * 128 + tid];
    reinterpret_cast<uint16_t*>(&lds[L_QB])[tid] = (uint16_t)(__builtin_bit_cast(unsigned, bfr(s)) >> 16);
  }
  SCHED_FENCE();
  __syncthreads();
  ISSUE(KA, VA, 0);
  ISSUE(KB, VB, 1);
  SCHED_FENCE();
  const u32x4 q = *reinterpret_cast<const u32x4*>(&lds[L_QB + 4 * c]);

  float m = -INFINITY, l = 0.f, acc[8];
#pragma unroll
  for (int j = 0; j < 8; ++j) acc[j] = 0.f;
#define CONSUME(KR, VR) CONSUMEN(KR, VR, CL)
#define CONSUMEN(KR, VR, CL) do { \
    float s_[CL]; float mn_ = m; \
    _Pragma("unroll") for (int i_ = 0; i_ < CL; ++i_) { \
      float d_ = dot2(KR[i_][0], q[0], 0.f); d_ = dot2(KR[i_][1], q[1], d_); d_ = dot2(KR[i_][2], q[2], d_); d_ = dot2(KR[i_][3], q[3], d_); \
      d_ = row16_sum(d_); s_[i_] = bfr(bfr(d_) * SCALE) * LOG2E; mn_ = fmaxf(mn_, s_[i_]); } \
    const float al_ = __builtin_amdgcn_exp2f(m - mn_); m = mn_; \
    float ps_ = 0.f; \
    _Pragma("unroll") for (int i_ = 0; i_ < CL; ++i_) { s_[i_] = __builtin_amdgcn_exp2f(s_[i_] - mn_); ps_ += s_[i_]; } \
    l = fmaf(l, al_, ps_); \
    _Pragma("unroll") for (int j_ = 0; j_ < 8; ++j_) acc[j_] *= al_; \
    _Pragma("unroll") for (int i_ = 0; i_ < CL; ++i_) { \
      _Pragma("unroll") for (int j_ = 0; j_ < 4; ++j_) { \
        acc[2 * j_] = fmaf(s_[i_], bf_lo(VR[i_][j_]), acc[2 * j_]); \
        acc[2 * j_ + 1] = fmaf(s_[i_], bf_hi(VR[i_][j_]), acc[2 * j_ + 1]); } } \
    asm volatile("" : "+v"(acc[0]), "+v"(acc[1]), "+v"(acc[2]), "+v"(acc[3]), "+v"(acc[4]), "+v"(acc[5]), "+v"(acc[6]), "+v"(acc[7]), "+v"(l), "+v"(m)); \
  } while (0)
#define STEP2(it) do { \
    CONSUME(KA, VA); SCHED_FENCE(); ISSUE(KA, VA, (it) + 2); SCHED_FENCE(); \
    CONSUME(KB, VB); SCHED_FENCE(); ISSUE(KB, VB, (it) + 3); SCHED_FENCE(); } while (0)

  if (w >= QW) {
#pragma unroll
    for (int i = 0; i < IL; i += 4) { CONSUMEN((KI + i), (VI + i), 4); SCHED_FENCE(); }
  }
  const int it_w = STAG * w;
#pragma unroll 1
  for (int it = 0; it < it_w; it += 2) STEP2(it);
  const int c0 = (FS * h) >> 3;
  const int foff = FS * h - 8 * c0;
  u32x4 gv[FR], uv[FR];
  {
    const unsigned voff = (unsigned)(FR * r8) * (FF * 2) + 16 * min(c0 + c8, FF / 8 - 1);
    const char* gb = reinterpret_cast<const char*>(wg) + (size_t)(RW * w) * (FF * 2);
    const char* ub = reinterpret_cast<const char*>(wu) + (size_t)(RW * w) * (FF * 2);
#pragma unroll
    for (int i = 0; i < FR; ++i) { gv[i] = ldw(gb + (size_t)i * (FF * 2) + voff); uv[i] = ldw(ub + (size_t)i * (FF * 2) + voff); }
  }
  SCHED_FENCE();
  STEP2(it_w);
  {
    const u32x4 xa = *reinterpret_cast<const u32x4*>(xn16 + RW * w + FR * r8);
    float ag[8], au[8];
#pragma unroll
    for (int j = 0; j < 8; ++j) { ag[j] = 0.f; au[j] = 0.f; }
#pragma unroll
    for (int i = 0; i < FR; ++i) {
      const unsigned p = xa[i >> 1];
      const unsigned slo = (i & 1) ? (p >> 16) : (p & 0xffffu), shi = (i & 1) ? (p & 0xffff0000u) : (p << 16);
      axpy8(ag, gv[i], slo, shi);
      axpy8(au, uv[i], slo, shi);
    }
#pragma unroll
    for (int j = 0; j < 8; ++j) {
      ag[j] += dpp<ROR8>(ag[j]); ag[j] = xrow_sum(ag[j]);
      au[j] += dpp<ROR8>(au[j]); au[j] = xrow_sum(au[j]);
    }
    if (r8 == 0) {
      float* fp = &lds[L_FP + w * 128 + 8 * c8];
      *reinterpret_cast<f32x4*>(fp) = f32x4{ag[0], ag[1], ag[2], ag[3]};
      *reinterpret_cast<f32x4*>(fp + 4) = f32x4{ag[4], ag[5], ag[6], ag[7]};
      *reinterpret_cast<f32x4*>(fp + 64) = f32x4{au[0], au[1], au[2], au[3]};
      *reinterpret_cast<f32x4*>(fp + 68) = f32x4{au[4], au[5], au[6], au[7]};
    }
  }
  SCHED_FENCE();
  u32x4 wov[OR], wdv[DR];
  {
    const char* ob = reinterpret_cast<const char*>(wo) + (size_t)(h * DH + OR * w) * (DM * 2);
    const char* db = reinterpret_cast<const char*>(wd) + (size_t)(FS * h) * (DM * 2);
#pragma unroll
    for (int i = 0; i < OR; ++i) wov[i] = ldw(ob + i * (DM * 2) + loff);
#pragma unroll
    for (int i = 0; i < DR; ++i) wdv[i] = ldw(db + (size_t)min(w + NW * i, FS - 1) * (DM * 2) + loff);
  }
  SCHED_FENCE();
#pragma unroll 1
  for (int it = it_w + 2; it < NCH - 2; it += 2) STEP2(it);
  CONSUME(KA, VA);
  SCHED_FENCE();
  CONSUME(KB, VB);
#undef STEP2
#undef CONSUME
#undef CONSUMEN
#undef ISSUE

  {
    const float M = xrow_max(m);
    const float wt = __builtin_amdgcn_exp2f(m - M);
    l = xrow_sum(l * wt);
#pragma unroll
    for (int j = 0; j < 8; ++j) acc[j] = xrow_sum(acc[j] * wt);
    if (r == 0) {
      float* rec = &lds[L_REC + w * 132];
      *reinterpret_cast<f32x4*>(rec + 4 + 8 * c) = f32x4{acc[0], acc[1], acc[2], acc[3]};
      *reinterpret_cast<f32x4*>(rec + 8 + 8 * c) = f32x4{acc[4], acc[5], acc[6], acc[7]};
      if (c == 0) *reinterpret_cast<f32x2*>(rec) = f32x2{M, l};
    }
  }
  SCHED_FENCE();
  __syncthreads();
  SCHED_FENCE();
  {
    f32x2 ml[NW];
    float av[NW], g = 0.f, u = 0.f;
    const int jr = min(w + NW * (lane & 7), FS - 1);
#pragma unroll
    for (int i = 0; i < NW; ++i) {
      ml[i] = *reinterpret_cast<const f32x2*>(&lds[L_REC + i * 132]);
      av[i] = lds[L_REC + i * 132 + 4 + OR * w + c];
      g += lds[L_FP + i * 128 + foff + jr];
      u += lds[L_FP + i * 128 + 64 + foff + jr];
    }
    float M = ml[0][0];
#pragma unroll
    for (int i = 1; i < NW; ++i) M = fmaxf(M, ml[i][0]);
    float L = 0.f, o = 0.f;
#pragma unroll
    for (int i = 0; i < NW; ++i) {
      const float wt = __builtin_amdgcn_exp2f(ml[i][0] - M);
      L = fmaf(wt, ml[i][1], L);
      o = fmaf(wt, av[i], o);
    }
    const unsigned ob = __builtin_bit_cast(unsigned, bfr(o / L));
    g = bfr(g); u = bfr(u);
    g = g > 0.f ? g : 0.f;
    const unsigned hb = __builtin_bit_cast(unsigned, bfr(g * u));
    float a[8], bb[8];
#pragma unroll
    for (int j = 0; j < 8; ++j) { a[j] = 0.f; bb[j] = 0.f; }
#pragma unroll
    for (int i = 0; i < OR; ++i) {
      const unsigned s = __builtin_amdgcn_readlane(ob, i);
      axpy8(a, wov[i], s >> 16, s);
    }
#pragma unroll
    for (int t = 0; t < DR; ++t) {
      unsigned s = __builtin_amdgcn_readlane(hb, t);
      if (w + NW * t >= FS) s = 0u;
      axpy8(bb, wdv[t], s >> 16, s);
    }
    *reinterpret_cast<f32x4*>(&lds[L_RA + w * 512 + 8 * lane]) = f32x4{a[0], a[1], a[2], a[3]};
    *reinterpret_cast<f32x4*>(&lds[L_RA + w * 512 + 8 * lane + 4]) = f32x4{a[4], a[5], a[6], a[7]};
    *reinterpret_cast<f32x4*>(&lds[L_RB + w * 512 + 8 * lane]) = f32x4{bb[0], bb[1], bb[2], bb[3]};
    *reinterpret_cast<f32x4*>(&lds[L_RB + w * 512 + 8 * lane + 4]) = f32x4{bb[4], bb[5], bb[6], bb[7]};
  }
  __syncthreads();
  {
    float sa = 0.f, sb = 0.f;
#pragma unroll
    for (int i = 0; i < NW; ++i) { sa += lds[L_RA + i * 512 + tid]; sb += lds[L_RB + i * 512 + tid]; }
    pa[(size_t)(b * NH + h) * DM + tid] = sa;
    pb[(size_t)(b * NH + h) * DM + tid] = sb;
  }
}

__global__ void __launch_bounds__(128) reduce_kernel(const uint16_t* __restrict__ x, const float* __restrict__ pa,
                                                     const float* __restrict__ pb, uint16_t* __restrict__ out) {
  const int idx = blockIdx.x * 128 + threadIdx.x;
  const int b = idx >> 9, n = idx & 511;
  float va[NH], vb[NH];
#pragma unroll
  for (int i = 0; i < NH; ++i) { va[i] = pa[(size_t)(b * NH + i) * DM + n]; vb[i] = pb[(size_t)(b * NH + i) * DM + n]; }
  float sa = 0.f, sb = 0.f;
#pragma unroll
  for (int i = 0; i < NH; ++i) { sa += va[i]; sb += vb[i]; }
  const float xv = __builtin_bit_cast(float, (unsigned)x[idx] << 16);
  const float o = bfr(bfr(xv + bfr(sa)) + bfr(sb));
  out[idx] = (uint16_t)(__builtin_bit_cast(unsigned, o) >> 16);
}

extern "C" void kernel_launch(void* const* d_in, const int* in_sizes, int n_in, void* d_out, int out_size,
                              void* d_ws, size_t ws_size, hipStream_t stream) {
  const uint16_t* x = (const uint16_t*)d_in[0];
  const uint16_t* kc = (const uint16_t*)d_in[1];
  const uint16_t* vc = (const uint16_t*)d_in[2];
  const uint16_t* wq = (const uint16_t*)d_in[3];
  const uint16_t* wo = (const uint16_t*)d_in[4];
  const uint16_t* wg = (const uint16_t*)d_in[6];
  const uint16_t* wu = (const uint16_t*)d_in[7];
  const uint16_t* wd = (const uint16_t*)d_in[8];
  float* pa = (float*)d_ws;
  float* pb = pa + NB * NH * DM;
  fused_decode_kernel<<<NB * NH, NT, 0, stream>>>(x, kc, vc, wq, wo, wg, wu, wd, pa, pb);
  reduce_kernel<<<NB * DM / 128, 128, 0, stream>>>(x, pa, pb, (uint16_t*)d_out);
}
```

```cpp
#define CFG_IB 96
#define CFG_D 8
#include <hip/hip_runtime.h>
#include <hip/hip_bf16.h>
#include <stdint.h>

typedef unsigned u32x4 __attribute__((ext_vector_type(4)));
typedef unsigned u32x2 __attribute__((ext_vector_type(2)));
typedef float f32x4 __attribute__((ext_vector_type(4)));
typedef float f32x2 __attribute__((ext_vector_type(2)));
typedef __bf16 bf16x2 __attribute__((ext_vector_type(2)));

constexpr int NB = 8, DM = 512, NH = 32, SK = 4096, DH = 128, FF = 1376, FS = 43;
constexpr int NW = 8, NT = NW * 64;
#define CFG_CK 4
constexpr int CK = CFG_CK;
constexpr int CL = CK / 4;
constexpr int IB = CFG_IB;
constexpr int IL = IB / 4;
constexpr int NCHT = (SK - 4 * IB) / CK;
constexpr int NQ = NCHT / 8 + CFG_D / 2;
constexpr int NK = NCHT / 8 - CFG_D / 2;
#define CFG_NS 64
constexpr int NS = CFG_NS;
#define CFG_STAG (32 / CFG_CK)
constexpr int STAG = CFG_STAG;
constexpr int RW = DM / NW;
constexpr int QW = 4;
constexpr int QRW = DM / QW;
constexpr int QR = QRW / 4;
constexpr int FR = RW / 8;
constexpr int OR = DH / NW;
constexpr int DR = (FS + NW - 1) / NW;
constexpr float SCALE = 0.08838834764831845f;
constexpr float LOG2E = 1.4426950408889634f;
static_assert(STAG % 2 == 0 && STAG * (NW - 1) + 6 <= NS && NS % 2 == 0 && NS + 2 <= NK && NQ % 2 == 0 && NK % 2 == 0 && 4 * (NQ + NK) == NCHT && RW == 64, "config");

constexpr int L_XN = 0;
constexpr int L_QP = L_XN + NW * 32;
constexpr int L_QB = L_QP + NW * 128;
constexpr int L_FP = L_QB + 64;
constexpr int L_REC = L_FP + NW * 128;
constexpr int L_RA = L_REC + NW * 132;
constexpr int L_RB = L_RA + NW * 512;
constexpr int L_CT = L_RB + NW * 512;
constexpr int L_END = L_CT + 4;

__device__ __forceinline__ float bfr(float x) { return __bfloat162float(__float2bfloat16(x)); }
__device__ __forceinline__ float bf_lo(unsigned u) { return __builtin_bit_cast(float, u << 16); }
__device__ __forceinline__ float bf_hi(unsigned u) { return __builtin_bit_cast(float, u & 0xffff0000u); }
__device__ __forceinline__ float dot2(unsigned k, unsigned q, float acc) {
  return __builtin_amdgcn_fdot2_f32_bf16(__builtin_bit_cast(bf16x2, k), __builtin_bit_cast(bf16x2, q), acc, false);
}
__device__ __forceinline__ void axpy8(float (&acc)[8], const u32x4& row, unsigned slo, unsigned shi) {
#pragma unroll
  for (int j = 0; j < 4; ++j) { acc[2 * j] = dot2(row[j], slo, acc[2 * j]); acc[2 * j + 1] = dot2(row[j], shi, acc[2 * j + 1]); }
}
template <int CTRL> __device__ __forceinline__ float dpp(float x) {
  return __builtin_bit_cast(float, __builtin_amdgcn_mov_dpp(__builtin_bit_cast(int, x), CTRL, 0xf, 0xf, true));
}
constexpr int ROR1 = 0x121, ROR2 = 0x122, ROR4 = 0x124, ROR8 = 0x128;
__device__ __forceinline__ float row16_sum(float x) {
  x += dpp<ROR8>(x); x += dpp<ROR4>(x); x += dpp<ROR2>(x); x += dpp<ROR1>(x); return x;
}
__device__ __forceinline__ float xrow_sum(float x) {
  auto s = __builtin_amdgcn_permlane16_swap(__float_as_uint(x), __float_as_uint(x), false, false);
  x = __uint_as_float(s[0]) + __uint_as_float(s[1]);
  auto t = __builtin_amdgcn_permlane32_swap(__float_as_uint(x), __float_as_uint(x), false, false);
  return __uint_as_float(t[0]) + __uint_as_float(t[1]);
}
__device__ __forceinline__ float xrow_max(float x) {
  auto s = __builtin_amdgcn_permlane16_swap(__float_as_uint(x), __float_as_uint(x), false, false);
  x = fmaxf(__uint_as_float(s[0]), __uint_as_float(s[1]));
  auto t = __builtin_amdgcn_permlane32_swap(__float_as_uint(x), __float_as_uint(x), false, false);
  return fmaxf(__uint_as_float(t[0]), __uint_as_float(t[1]));
}
__device__ __forceinline__ u32x4 ldnt(const void* p) { return __builtin_nontemporal_load(reinterpret_cast<const u32x4*>(p)); }
__device__ __forceinline__ u32x4 ld16(const void* p) { return *reinterpret_cast<const u32x4*>(p); }
__device__ __forceinline__ u32x4 ldw(const void* p) { return ld16(p); }
#define SCHED_FENCE() __builtin_amdgcn_sched_barrier(0)
#define MEM_FENCE() asm volatile("" ::: "memory")

__global__ void __launch_bounds__(NT) fused_decode_kernel(
    const uint16_t* __restrict__ x, const uint16_t* __restrict__ kc, const uint16_t* __restrict__ vc,
    const uint16_t* __restrict__ wq, const uint16_t* __restrict__ wo, const uint16_t* __restrict__ wg,
    const uint16_t* __restrict__ wu, const uint16_t* __restrict__ wd, float* __restrict__ pa,
    float* __restrict__ pb) {
  __shared__ __attribute__((aligned(16))) float lds[L_END];
  const int h = blockIdx.x & (NH - 1), b = blockIdx.x >> 5;
  const int tid = threadIdx.x, lane = tid & 63, w = __builtin_amdgcn_readfirstlane(tid >> 6);
  const int r = lane >> 4, c = lane & 15, r8 = lane >> 3, c8 = lane & 7;

  const char* kbase = reinterpret_cast<const char*>(kc) + (size_t)(b * NH + h) * (SK * DH * 2) + (size_t)(4 * IB) * (DH * 2);
  const char* vbase = reinterpret_cast<const char*>(vc) + (size_t)(b * NH + h) * (SK * DH * 2) + (size_t)(4 * IB) * (DH * 2);
  u32x4 KI[IL], VI[IL];
  const unsigned loff = lane * 16;
  constexpr size_t CSTRIDE = (size_t)CK * DH * 2;
  unsigned* ctr = reinterpret_cast<unsigned*>(&lds[L_CT]);
  u32x4 KA[CL], VA[CL], KB[CL], VB[CL];
#define ISSUE(KR, VR, ch) do { _Pragma("unroll") for (int i_ = 0; i_ < CL; ++i_) KR[i_] = ldnt(kbase + (size_t)(ch) * CSTRIDE + i_ * 1024 + loff); \
                               _Pragma("unroll") for (int i_ = 0; i_ < CL; ++i_) VR[i_] = ldnt(vbase + (size_t)(ch) * CSTRIDE + i_ * 1024 + loff); } while (0)
  uint16_t* xn16 = reinterpret_cast<uint16_t*>(&lds[L_XN]);
  if (tid == NT - 1) ctr[0] = 0u;
  if (w < QW) {
    const uint16_t* xb = x + b * DM;
    const u32x4 xall = ld16(xb + 8 * lane);
    const unsigned xm0 = xb[QRW * w + lane], xm1 = xb[QRW * w + 64 + lane];
    u32x4 wqv[QR];
    {
      const char* base = reinterpret_cast<const char*>(wq) + (size_t)(QRW * w) * (NH * DH * 2) + h * (DH * 2);
      const unsigned voff = (unsigned)(QR * r) * (NH * DH * 2) + 16 * c;
#pragma unroll
      for (int i = 0; i < QR; ++i) wqv[i] = ldw(base + (size_t)i * (NH * DH * 2) + voff);
    }
    SCHED_FENCE();
    __builtin_amdgcn_s_barrier();
    SCHED_FENCE();
    {
      float xs[8];
#pragma unroll
      for (int j = 0; j < 4; ++j) { xs[2 * j] = bf_lo(xall[j]); xs[2 * j + 1] = bf_hi(xall[j]); }
      float s = ((xs[0] + xs[1]) + (xs[2] + xs[3])) + ((xs[4] + xs[5]) + (xs[6] + xs[7]));
      s = xrow_sum(row16_sum(s));
      const float mean = s * (1.0f / DM);
      float v = 0.f;
#pragma unroll
      for (int j = 0; j < 8; ++j) { const float d = xs[j] - mean; v = fmaf(d, d, v); }
      v = xrow_sum(row16_sum(v));
      const float var_b = bfr(v * (1.0f / DM));
      const float mean_b = bfr(mean);
      const float den = bfr(sqrtf(bfr(var_b + 1e-5f)));
      const float xn0 = bfr(bfr(bf_lo(xm0) - mean_b) / den);
      const float xn1 = bfr(bfr(bf_lo(xm1) - mean_b) / den);
      xn16[QRW * w + lane] = (uint16_t)(__builtin_bit_cast(unsigned, xn0) >> 16);
      xn16[QRW * w + 64 + lane] = (uint16_t)(__builtin_bit_cast(unsigned, xn1) >> 16);
    }
    u32x4 xa[QR / 8];
#pragma unroll
    for (int i = 0; i < QR / 8; ++i) xa[i] = *reinterpret_cast<const u32x4*>(xn16 + QRW * w + QR * r + 8 * i);
    float acc[8];
#pragma unroll
    for (int j = 0; j < 8; ++j) acc[j] = 0.f;
#pragma unroll
    for (int i = 0; i < QR; ++i) {
      const unsigned p = xa[i >> 3][(i >> 1) & 3];
      axpy8(acc, wqv[i], (i & 1) ? (p >> 16) : (p & 0xffffu), (i & 1) ? (p & 0xffff0000u) : (p << 16));
    }
#pragma unroll
    for (int j = 0; j < 8; ++j) acc[j] = xrow_sum(acc[j]);
    if (r == 0) {
      *reinterpret_cast<f32x4*>(&lds[L_QP + w * 128 + 8 * c]) = f32x4{acc[0], acc[1], acc[2], acc[3]};
      *reinterpret_cast<f32x4*>(&lds[L_QP + w * 128 + 8 * c + 4]) = f32x4{acc[4], acc[5], acc[6], acc[7]};
    }
  } else {
    __builtin_amdgcn_s_barrier();
    SCHED_FENCE();
    const char* ki = reinterpret_cast<const char*>(kc) + (size_t)(b * NH + h) * (SK * DH * 2) + (size_t)((w - QW) * IB) * (DH * 2);
    const char* vi = reinterpret_cast<const char*>(vc) + (size_t)(b * NH + h) * (SK * DH * 2) + (size_t)((w - QW) * IB) * (DH * 2);
#pragma unroll
    for (int i = 0; i < IL; ++i) KI[i] = ldnt(ki + i * 1024 + loff);
#pragma unroll
    for (int i = 0; i < IL; ++i) VI[i] = ldnt(vi + i * 1024 + loff);
  }
  SCHED_FENCE();
  __syncthreads();
  if (tid < 128) {
    float s = 0.f;
#pragma unroll
    for (int i = 0; i < QW; ++i) s += lds[L_QP + i * 128 + tid];
    reinterpret_cast<uint16_t*>(&lds[L_QB])[tid] = (uint16_t)(__builtin_bit_cast(unsigned, bfr(s)) >> 16);
  }
  SCHED_FENCE();
  __syncthreads();
  ISSUE(KA, VA, w);
  ISSUE(KB, VB, NW + w);
  SCHED_FENCE();
  const u32x4 q = *reinterpret_cast<const u32x4*>(&lds[L_QB + 4 * c]);

  float m = -INFINITY, l = 0.f, acc[8];
#pragma unroll
  for (int j = 0; j < 8; ++j) acc[j] = 0.f;
#define CONSUME(KR, VR) CONSUMEN(KR, VR, CL)
#define CONSUMEN(KR, VR, CL) do { \
    float s_[CL]; float mn_ = m; \
    _Pragma("unroll") for (int i_ = 0; i_ < CL; ++i_) { \
      float d_ = dot2(KR[i_][0], q[0], 0.f); d_ = dot2(KR[i_][1], q[1], d_); d_ = dot2(KR[i_][2], q[2], d_); d_ = dot2(KR[i_][3], q[3], d_); \
      d_ = row16_sum(d_); s_[i_] = bfr(bfr(d_) * SCALE) * LOG2E; mn_ = fmaxf(mn_, s_[i_]); } \
    const float al_ = __builtin_amdgcn_exp2f(m - mn_); m = mn_; \
    float ps_ = 0.f; \
    _Pragma("unroll") for (int i_ = 0; i_ < CL; ++i_) { s_[i_] = __builtin_amdgcn_exp2f(s_[i_] - mn_); ps_ += s_[i_]; } \
    l = fmaf(l, al_, ps_); \
    _Pragma("unroll") for (int j_ = 0; j_ < 8; ++j_) acc[j_] *= al_; \
    _Pragma("unroll") for (int i_ = 0; i_ < CL; ++i_) { \
      _Pragma("unroll") for (int j_ = 0; j_ < 4; ++j_) { \
        acc[2 * j_] = fmaf(s_[i_], bf_lo(VR[i_][j_]), acc[2 * j_]); \
        acc[2 * j_ + 1] = fmaf(s_[i_], bf_hi(VR[i_][j_]), acc[2 * j_ + 1]); } } \
    asm volatile("" : "+v"(acc[0]), "+v"(acc[1]), "+v"(acc[2]), "+v"(acc[3]), "+v"(acc[4]), "+v"(acc[5]), "+v"(acc[6]), "+v"(acc[7]), "+v"(l), "+v"(m)); \
  } while (0)
#define CIDX(i) (((i) < NK ? NW * (i) : (NW - QW) * NK + QW * (i)) + w)
#define STEP2(it) do { \
    CONSUME(KA, VA); SCHED_FENCE(); ISSUE(KA, VA, CIDX((it) + 2)); SCHED_FENCE(); \
    CONSUME(KB, VB); SCHED_FENCE(); ISSUE(KB, VB, CIDX((it) + 3)); SCHED_FENCE(); } while (0)

  if (w >= QW) {
#pragma unroll
    for (int i = 0; i < IL; i += 4) { CONSUMEN((KI + i), (VI + i), 4); SCHED_FENCE(); }
  }
  const int it_w = STAG * w;
#pragma unroll 1
  for (int it = 0; it < it_w; it += 2) STEP2(it);
  const int c0 = (FS * h) >> 3;
  const int foff = FS * h - 8 * c0;
  u32x4 gv[FR], uv[FR];
  {
    const unsigned voff = (unsigned)(FR * r8) * (FF * 2) + 16 * min(c0 + c8, FF / 8 - 1);
    const char* gb = reinterpret_cast<const char*>(wg) + (size_t)(RW * w) * (FF * 2);
    const char* ub = reinterpret_cast<const char*>(wu) + (size_t)(RW * w) * (FF * 2);
#pragma unroll
    for (int i = 0; i < FR; ++i) { gv[i] = ldw(gb + (size_t)i * (FF * 2) + voff); uv[i] = ldw(ub + (size_t)i * (FF * 2) + voff); }
  }
  SCHED_FENCE();
  STEP2(it_w);
  {
    const u32x4 xa = *reinterpret_cast<const u32x4*>(xn16 + RW * w + FR * r8);
    float ag[8], au[8];
#pragma unroll
    for (int j = 0; j < 8; ++j) { ag[j] = 0.f; au[j] = 0.f; }
#pragma unroll
    for (int i = 0; i < FR; ++i) {
      const unsigned p = xa[i >> 1];
      const unsigned slo = (i & 1) ? (p >> 16) : (p & 0xffffu), shi = (i & 1) ? (p & 0xffff0000u) : (p << 16);
      axpy8(ag, gv[i], slo, shi);
      axpy8(au, uv[i], slo, shi);
    }
#pragma unroll
    for (int j = 0; j < 8; ++j) {
      ag[j] += dpp<ROR8>(ag[j]); ag[j] = xrow_sum(ag[j]);
      au[j] += dpp<ROR8>(au[j]); au[j] = xrow_sum(au[j]);
    }
    if (r8 == 0) {
      float* fp = &lds[L_FP + w * 128 + 8 * c8];
      *reinterpret_cast<f32x4*>(fp) = f32x4{ag[0], ag[1], ag[2], ag[3]};
      *reinterpret_cast<f32x4*>(fp + 4) = f32x4{ag[4], ag[5], ag[6], ag[7]};
      *reinterpret_cast<f32x4*>(fp + 64) = f32x4{au[0], au[1], au[2], au[3]};
      *reinterpret_cast<f32x4*>(fp + 68) = f32x4{au[4], au[5], au[6], au[7]};
    }
    MEM_FENCE();
    if (lane == 0) __hip_atomic_fetch_add(&ctr[0], 1u, __ATOMIC_RELAXED, __HIP_MEMORY_SCOPE_WORKGROUP);
    MEM_FENCE();
  }
  SCHED_FENCE();
  u32x4 wov[OR], wdv[DR];
  {
    const char* ob = reinterpret_cast<const char*>(wo) + (size_t)(h * DH + OR * w) * (DM * 2);
    const char* db = reinterpret_cast<const char*>(wd) + (size_t)(FS * h) * (DM * 2);
#pragma unroll
    for (int i = 0; i < OR; ++i) wov[i] = ldw(ob + i * (DM * 2) + loff);
#pragma unroll
    for (int i = 0; i < DR; ++i) wdv[i] = ldw(db + (size_t)min(w + NW * i, FS - 1) * (DM * 2) + loff);
  }
  SCHED_FENCE();
  {
    int it = it_w + 2;
#pragma unroll 1
    do { STEP2(it); it += 2; } while (it < NS - 2);
  }
  {
    while (__hip_atomic_load(&ctr[0], __ATOMIC_RELAXED, __HIP_MEMORY_SCOPE_WORKGROUP) < (unsigned)NW) __builtin_amdgcn_s_sleep(1);
    MEM_FENCE();
    float g = 0.f, u = 0.f;
    const int jr = min(w + NW * (lane & 7), FS - 1);
#pragma unroll
    for (int i = 0; i < NW; ++i) {
      g += lds[L_FP + i * 128 + foff + jr];
      u += lds[L_FP + i * 128 + 64 + foff + jr];
    }
    g = bfr(g); u = bfr(u);
    g = g > 0.f ? g : 0.f;
    const unsigned hb = __builtin_bit_cast(unsigned, bfr(g * u));
    float bb[8];
#pragma unroll
    for (int j = 0; j < 8; ++j) bb[j] = 0.f;
#pragma unroll
    for (int t = 0; t < DR; ++t) {
      unsigned s = __builtin_amdgcn_readlane(hb, t);
      if (w + NW * t >= FS) s = 0u;
      axpy8(bb, wdv[t], s >> 16, s);
    }
    *reinterpret_cast<f32x4*>(&lds[L_RB + w * 512 + 8 * lane]) = f32x4{bb[0], bb[1], bb[2], bb[3]};
    *reinterpret_cast<f32x4*>(&lds[L_RB + w * 512 + 8 * lane + 4]) = f32x4{bb[4], bb[5], bb[6], bb[7]};
#pragma unroll
    for (int i = 0; i < OR; i += 2) {
#pragma unroll
      for (int j = 0; j < 4; ++j) {
        const unsigned r0 = wov[i][j], r1 = wov[i + 1][j];
        wov[i][j] = __builtin_amdgcn_perm(r1, r0, 0x05040100u);
        wov[i + 1][j] = __builtin_amdgcn_perm(r1, r0, 0x07060302u);
      }
      asm volatile("" : "+v"(wov[i]), "+v"(wov[i + 1]));
    }
  }
  SCHED_FENCE();
  {
    const int n_w = w < QW ? NQ : NK;
    int it = NS - 2;
#pragma unroll 1
    do { STEP2(it); it += 2; } while (it < n_w - 2);
  }
  CONSUME(KA, VA);
  SCHED_FENCE();
  CONSUME(KB, VB);
#undef STEP2
#undef CIDX
#undef CONSUME
#undef CONSUMEN
#undef ISSUE

  {
    const float M = xrow_max(m);
    const float wt = __builtin_amdgcn_exp2f(m - M);
    l = xrow_sum(l * wt);
#pragma unroll
    for (int j = 0; j < 8; ++j) acc[j] = xrow_sum(acc[j] * wt);
    if (r == 0) {
      float* rec = &lds[L_REC + w * 132];
      *reinterpret_cast<f32x4*>(rec + 4 + 8 * c) = f32x4{acc[0], acc[1], acc[2], acc[3]};
      *reinterpret_cast<f32x4*>(rec + 8 + 8 * c) = f32x4{acc[4], acc[5], acc[6], acc[7]};
      if (c == 0) *reinterpret_cast<f32x2*>(rec) = f32x2{M, l};
    }
  }
  SCHED_FENCE();
  __syncthreads();
  SCHED_FENCE();
  {
    f32x2 ml[NW];
    float av[NW];
#pragma unroll
    for (int i = 0; i < NW; ++i) {
      ml[i] = *reinterpret_cast<const f32x2*>(&lds[L_REC + i * 132]);
      av[i] = lds[L_REC + i * 132 + 4 + OR * w + c];
    }
    float M = ml[0][0];
#pragma unroll
    for (int i = 1; i < NW; ++i) M = fmaxf(M, ml[i][0]);
    float L = 0.f, o = 0.f;
#pragma unroll
    for (int i = 0; i < NW; ++i) {
      const float wt = __builtin_amdgcn_exp2f(ml[i][0] - M);
      L = fmaf(wt, ml[i][1], L);
      o = fmaf(wt, av[i], o);
    }
    const unsigned ob = __builtin_bit_cast(unsigned, bfr(o / L));
    const unsigned nx = __builtin_amdgcn_mov_dpp(ob, 0x101  , 0xf, 0xf, true);
    const unsigned op = __builtin_amdgcn_perm(nx, ob, 0x07060302u);
    float a[8];
#pragma unroll
    for (int j = 0; j < 8; ++j) a[j] = 0.f;
#pragma unroll
    for (int i = 0; i < OR; i += 2) {
      const unsigned s = __builtin_amdgcn_readlane(op, i);
#pragma unroll
      for (int j = 0; j < 4; ++j) { a[2 * j] = dot2(wov[i][j], s, a[2 * j]); a[2 * j + 1] = dot2(wov[i + 1][j], s, a[2 * j + 1]); }
    }
    *reinterpret_cast<f32x4*>(&lds[L_RA + w * 512 + 8 * lane]) = f32x4{a[0], a[1], a[2], a[3]};
    *reinterpret_cast<f32x4*>(&lds[L_RA + w * 512 + 8 * lane + 4]) = f32x4{a[4], a[5], a[6], a[7]};
  }
  __syncthreads();
  {
    float sa = 0.f, sb = 0.f;
#pragma unroll
    for (int i = 0; i < NW; ++i) { sa += lds[L_RA + i * 512 + tid]; sb += lds[L_RB + i * 512 + tid]; }
    pa[(size_t)(b * NH + h) * DM + tid] = sa;
    pb[(size_t)(b * NH + h) * DM + tid] = sb;
  }
}

__global__ void __launch_bounds__(128) reduce_kernel(const uint16_t* __restrict__ x, const float* __restrict__ pa,
                                                     const float* __restrict__ pb, uint16_t* __restrict__ out) {
  const int idx = blockIdx.x * 128 + threadIdx.x;
  const int b = idx >> 9, n = idx & 511;
  float va[NH], vb[NH];
#pragma unroll
  for (int i = 0; i < NH; ++i) { va[i] = pa[(size_t)(b * NH + i) * DM + n]; vb[i] = pb[(size_t)(b * NH + i) * DM + n]; }
  float sa = 0.f, sb = 0.f;
#pragma unroll
  for (int i = 0; i < NH; ++i) { sa += va[i]; sb += vb[i]; }
  const float xv = __builtin_bit_cast(float, (unsigned)x[idx] << 16);
  const float o = bfr(bfr(xv + bfr(sa)) + bfr(sb));
  out[idx] = (uint16_t)(__builtin_bit_cast(unsigned, o) >> 16);
}

extern "C" void kernel_launch(void* const* d_in, const int* in_sizes, int n_in, void* d_out, int out_size,
                              void* d_ws, size_t ws_size, hipStream_t stream) {
  const uint16_t* x = (const uint16_t*)d_in[0];
  const uint16_t* kc = (const uint16_t*)d_in[1];
  const uint16_t* vc = (const uint16_t*)d_in[2];
  const uint16_t* wq = (const uint16_t*)d_in[3];
  const uint16_t* wo = (const uint16_t*)d_in[4];
  const uint16_t* wg = (const uint16_t*)d_in[6];
  const uint16_t* wu = (const uint16_t*)d_in[7];
  const uint16_t* wd = (const uint16_t*)d_in[8];
  float* pa = (float*)d_ws;
  float* pb = pa + NB * NH * DM;
  fused_decode_kernel<<<NB * NH, NT, 0, stream>>>(x, kc, vc, wq, wo, wg, wu, wd, pa, pb);
  reduce_kernel<<<NB * DM / 128, 128, 0, stream>>>(x, pa, pb, (uint16_t*)d_out);
}
```

```cpp
#define CFG_IB 96
#define CFG_D 4
#define CFG_IG 4
#include <hip/hip_runtime.h>
#include <hip/hip_bf16.h>
#include <stdint.h>

typedef unsigned u32x4 __attribute__((ext_vector_type(4)));
typedef unsigned u32x2 __attribute__((ext_vector_type(2)));
typedef float f32x4 __attribute__((ext_vector_type(4)));
typedef float f32x2 __attribute__((ext_vector_type(2)));
typedef __bf16 bf16x2 __attribute__((ext_vector_type(2)));

constexpr int NB = 8, DM = 512, NH = 32, SK = 4096, DH = 128, FF = 1376, FS = 43;
constexpr int NW = 8, NT = NW * 64;
#define CFG_CK 4
constexpr int CK = CFG_CK;
constexpr int CL = CK / 4;
constexpr int IB = CFG_IB;
constexpr int IL = IB / 4;
constexpr int IG = CFG_IG;
constexpr int NCHT = (SK - 4 * IB) / CK;
constexpr int NQ = NCHT / 8 + CFG_D / 2;
constexpr int NK = NCHT / 8 - CFG_D / 2;
#define CFG_NS 64
constexpr int NS = CFG_NS;
#define CFG_STAG (32 / CFG_CK)
constexpr int STAG = CFG_STAG;
constexpr int RW = DM / NW;
constexpr int QW = 4;
constexpr int QRW = DM / QW;
constexpr int QR = QRW / 4;
constexpr int FR = RW / 8;
constexpr int OR = DH / NW;
constexpr int DR = (FS + NW - 1) / NW;
constexpr float SCALE = 0.08838834764831845f;
constexpr float LOG2E = 1.4426950408889634f;
static_assert(STAG % 2 == 0 && STAG * (NW - 1) + 6 <= NS && NS % 2 == 0 && NS + 2 <= NK && NQ % 2 == 0 && NK % 2 == 0 && 4 * (NQ + NK) == NCHT && RW == 64 && IL % (2 * IG) == 0 && IL / IG <= STAG * QW, "config");

constexpr int L_XN = 0;
constexpr int L_QP = L_XN + NW * 32;
constexpr int L_QB = L_QP + NW * 128;
constexpr int L_FP = L_QB + 64;
constexpr int L_REC = L_FP + NW * 128;
constexpr int L_RA = L_REC + NW * 132;
constexpr int L_RB = L_RA + NW * 512;
constexpr int L_CT = L_RB + NW * 512;
constexpr int L_END = L_CT + 4;

__device__ __forceinline__ float bfr(float x) { return __bfloat162float(__float2bfloat16(x)); }
__device__ __forceinline__ float bf_lo(unsigned u) { return __builtin_bit_cast(float, u << 16); }
__device__ __forceinline__ float bf_hi(unsigned u) { return __builtin_bit_cast(float, u & 0xffff0000u); }
__device__ __forceinline__ float dot2(unsigned k, unsigned q, float acc) {
  return __builtin_amdgcn_fdot2_f32_bf16(__builtin_bit_cast(bf16x2, k), __builtin_bit_cast(bf16x2, q), acc, false);
}
__device__ __forceinline__ void axpy8(float (&acc)[8], const u32x4& row, unsigned slo, unsigned shi) {
#pragma unroll
  for (int j = 0; j < 4; ++j) { acc[2 * j] = dot2(row[j], slo, acc[2 * j]); acc[2 * j + 1] = dot2(row[j], shi, acc[2 * j + 1]); }
}
template <int CTRL> __device__ __forceinline__ float dpp(float x) {
  return __builtin_bit_cast(float, __builtin_amdgcn_mov_dpp(__builtin_bit_cast(int, x), CTRL, 0xf, 0xf, true));
}
constexpr int ROR1 = 0x121, ROR2 = 0x122, ROR4 = 0x124, ROR8 = 0x128;
__device__ __forceinline__ float row16_sum(float x) {
  x += dpp<ROR8>(x); x += dpp<ROR4>(x); x += dpp<ROR2>(x); x += dpp<ROR1>(x); return x;
}
__device__ __forceinline__ float xrow_sum(float x) {
  auto s = __builtin_amdgcn_permlane16_swap(__float_as_uint(x), __float_as_uint(x), false, false);
  x = __uint_as_float(s[0]) + __uint_as_float(s[1]);
  auto t = __builtin_amdgcn_permlane32_swap(__float_as_uint(x), __float_as_uint(x), false, false);
  return __uint_as_float(t[0]) + __uint_as_float(t[1]);
}
__device__ __forceinline__ float xrow_max(float x) {
  auto s = __builtin_amdgcn_permlane16_swap(__float_as_uint(x), __float_as_uint(x), false, false);
  x = fmaxf(__uint_as_float(s[0]), __uint_as_float(s[1]));
  auto t = __builtin_amdgcn_permlane32_swap(__float_as_uint(x), __float_as_uint(x), false, false);
  return fmaxf(__uint_as_float(t[0]), __uint_as_float(t[1]));
}
__device__ __forceinline__ u32x4 ldnt(const void* p) { return __builtin_nontemporal_load(reinterpret_cast<const u32x4*>(p)); }
__device__ __forceinline__ u32x4 ld16(const void* p) { return *reinterpret_cast<const u32x4*>(p); }
__device__ __forceinline__ u32x4 ldw(const void* p) { return ld16(p); }
#define SCHED_FENCE() __builtin_amdgcn_sched_barrier(0)
#define MEM_FENCE() asm volatile("" ::: "memory")

__global__ void __launch_bounds__(NT) fused_decode_kernel(
    const uint16_t* __restrict__ x, const uint16_t* __restrict__ kc, const uint16_t* __restrict__ vc,
    const uint16_t* __restrict__ wq, const uint16_t* __restrict__ wo, const uint16_t* __restrict__ wg,
    const uint16_t* __restrict__ wu, const uint16_t* __restrict__ wd, float* __restrict__ pa,
    float* __restrict__ pb) {
  __shared__ __attribute__((aligned(16))) float lds[L_END];
  const int h = blockIdx.x & (NH - 1), b = blockIdx.x >> 5;
  const int tid = threadIdx.x, lane = tid & 63, w = __builtin_amdgcn_readfirstlane(tid >> 6);
  const int r = lane >> 4, c = lane & 15, r8 = lane >> 3, c8 = lane & 7;

  const char* kbase = reinterpret_cast<const char*>(kc) + (size_t)(b * NH + h) * (SK * DH * 2) + (size_t)(4 * IB) * (DH * 2);
  const char* vbase = reinterpret_cast<const char*>(vc) + (size_t)(b * NH + h) * (SK * DH * 2) + (size_t)(4 * IB) * (DH * 2);
  u32x4 KI[IL], VI[IL];
  const unsigned loff = lane * 16;
  constexpr size_t CSTRIDE = (size_t)CK * DH * 2;
  unsigned* ctr = reinterpret_cast<unsigned*>(&lds[L_CT]);
  u32x4 KA[CL], VA[CL], KB[CL], VB[CL];
#define ISSUE(KR, VR, ch) do { _Pragma("unroll") for (int i_ = 0; i_ < CL; ++i_) KR[i_] = ldnt(kbase + (size_t)(ch) * CSTRIDE + i_ * 1024 + loff); \
                               _Pragma("unroll") for (int i_ = 0; i_ < CL; ++i_) VR[i_] = ldnt(vbase + (size_t)(ch) * CSTRIDE + i_ * 1024 + loff); } while (0)
  uint16_t* xn16 = reinterpret_cast<uint16_t*>(&lds[L_XN]);
  if (tid == NT - 1) ctr[0] = 0u;
  if (w < QW) {
    const uint16_t* xb = x + b * DM;
    const u32x4 xall = ld16(xb + 8 * lane);
    const unsigned xm0 = xb[QRW * w + lane], xm1 = xb[QRW * w + 64 + lane];
    u32x4 wqv[QR];
    {
      const char* base = reinterpret_cast<const char*>(wq) + (size_t)(QRW * w) * (NH * DH * 2) + h * (DH * 2);
      const unsigned voff = (unsigned)(QR * r) * (NH * DH * 2) + 16 * c;
#pragma unroll
      for (int i = 0; i < QR; ++i) wqv[i] = ldw(base + (size_t)i * (NH * DH * 2) + voff);
    }
    SCHED_FENCE();
    __builtin_amdgcn_s_barrier();
    SCHED_FENCE();
    {
      float xs[8];
#pragma unroll
      for (int j = 0; j < 4; ++j) { xs[2 * j] = bf_lo(xall[j]); xs[2 * j + 1] = bf_hi(xall[j]); }
      float s = ((xs[0] + xs[1]) + (xs[2] + xs[3])) + ((xs[4] + xs[5]) + (xs[6] + xs[7]));
      s = xrow_sum(row16_sum(s));
      const float mean = s * (1.0f / DM);
      float v = 0.f;
#pragma unroll
      for (int j = 0; j < 8; ++j) { const float d = xs[j] - mean; v = fmaf(d, d, v); }
      v = xrow_sum(row16_sum(v));
      const float var_b = bfr(v * (1.0f / DM));
      const float mean_b = bfr(mean);
      const float den = bfr(sqrtf(bfr(var_b + 1e-5f)));
      const float xn0 = bfr(bfr(bf_lo(xm0) - mean_b) / den);
      const float xn1 = bfr(bfr(bf_lo(xm1) - mean_b) / den);
      xn16[QRW * w + lane] = (uint16_t)(__builtin_bit_cast(unsigned, xn0) >> 16);
      xn16[QRW * w + 64 + lane] = (uint16_t)(__builtin_bit_cast(unsigned, xn1) >> 16);
    }
    u32x4 xa[QR / 8];
#pragma unroll
    for (int i = 0; i < QR / 8; ++i) xa[i] = *reinterpret_cast<const u32x4*>(xn16 + QRW * w + QR * r + 8 * i);
    float acc[8];
#pragma unroll
    for (int j = 0; j < 8; ++j) acc[j] = 0.f;
#pragma unroll
    for (int i = 0; i < QR; ++i) {
      const unsigned p = xa[i >> 3][(i >> 1) & 3];
      axpy8(acc, wqv[i], (i & 1) ? (p >> 16) : (p & 0xffffu), (i & 1) ? (p & 0xffff0000u) : (p << 16));
    }
#pragma unroll
    for (int j = 0; j < 8; ++j) acc[j] = xrow_sum(acc[j]);
    if (r == 0) {
      *reinterpret_cast<f32x4*>(&lds[L_QP + w * 128 + 8 * c]) = f32x4{acc[0], acc[1], acc[2], acc[3]};
      *reinterpret_cast<f32x4*>(&lds[L_QP + w * 128 + 8 * c + 4]) = f32x4{acc[4], acc[5], acc[6], acc[7]};
    }
  } else {
    __builtin_amdgcn_s_barrier();
    SCHED_FENCE();
    const char* ki = reinterpret_cast<const char*>(kc) + (size_t)(b * NH + h) * (SK * DH * 2) + (size_t)((w - QW) * IB) * (DH * 2);
    const char* vi = reinterpret_cast<const char*>(vc) + (size_t)(b * NH + h) * (SK * DH * 2) + (size_t)((w - QW) * IB) * (DH * 2);
#pragma unroll
    for (int i = 0; i < IL; ++i) KI[i] = ldnt(ki + i * 1024 + loff);
#pragma unroll
    for (int i = 0; i < IL; ++i) VI[i] = ldnt(vi + i * 1024 + loff);
  }
  SCHED_FENCE();
  __syncthreads();
  if (tid < 128) {
    float s = 0.f;
#pragma unroll
    for (int i = 0; i < QW; ++i) s += lds[L_QP + i * 128 + tid];
    reinterpret_cast<uint16_t*>(&lds[L_QB])[tid] = (uint16_t)(__builtin_bit_cast(unsigned, bfr(s)) >> 16);
  }
  SCHED_FENCE();
  __syncthreads();
  ISSUE(KA, VA, w);
  ISSUE(KB, VB, NW + w);
  SCHED_FENCE();
  const u32x4 q = *reinterpret_cast<const u32x4*>(&lds[L_QB + 4 * c]);

  float m = -INFINITY, l = 0.f, acc[8];
#pragma unroll
  for (int j = 0; j < 8; ++j) acc[j] = 0.f;
#define CONSUME(KR, VR) CONSUMEN(KR, VR, CL)
#define CONSUMEN(KR, VR, CL) do { \
    float s_[CL]; float mn_ = m; \
    _Pragma("unroll") for (int i_ = 0; i_ < CL; ++i_) { \
      float d_ = dot2(KR[i_][0], q[0], 0.f); d_ = dot2(KR[i_][1], q[1], d_); d_ = dot2(KR[i_][2], q[2], d_); d_ = dot2(KR[i_][3], q[3], d_); \
      d_ = row16_sum(d_); s_[i_] = bfr(bfr(d_) * SCALE) * LOG2E; mn_ = fmaxf(mn_, s_[i_]); } \
    const float al_ = __builtin_amdgcn_exp2f(m - mn_); m = mn_; \
    float ps_ = 0.f; \
    _Pragma("unroll") for (int i_ = 0; i_ < CL; ++i_) { s_[i_] = __builtin_amdgcn_exp2f(s_[i_] - mn_); ps_ += s_[i_]; } \
    l = fmaf(l, al_, ps_); \
    _Pragma("unroll") for (int j_ = 0; j_ < 8; ++j_) acc[j_] *= al_; \
    _Pragma("unroll") for (int i_ = 0; i_ < CL; ++i_) { \
      _Pragma("unroll") for (int j_ = 0; j_ < 4; ++j_) { \
        acc[2 * j_] = fmaf(s_[i_], bf_lo(VR[i_][j_]), acc[2 * j_]); \
        acc[2 * j_ + 1] = fmaf(s_[i_], bf_hi(VR[i_][j_]), acc[2 * j_ + 1]); } } \
    asm volatile("" : "+v"(acc[0]), "+v"(acc[1]), "+v"(acc[2]), "+v"(acc[3]), "+v"(acc[4]), "+v"(acc[5]), "+v"(acc[6]), "+v"(acc[7]), "+v"(l), "+v"(m)); \
  } while (0)
#define CIDX(i) (((i) < NK ? NW * (i) : (NW - QW) * NK + QW * (i)) + w)
#define STEP2(it) do { \
    CONSUME(KA, VA); SCHED_FENCE(); ISSUE(KA, VA, CIDX((it) + 2)); SCHED_FENCE(); \
    CONSUME(KB, VB); SCHED_FENCE(); ISSUE(KB, VB, CIDX((it) + 3)); SCHED_FENCE(); } while (0)

  int it0 = 0;
  if (w >= QW) {
#pragma unroll
    for (int i = 0; i < IL; i += 2 * IG) {
      CONSUMEN((KI + i), (VI + i), IG); SCHED_FENCE();
      CONSUME(KA, VA); SCHED_FENCE(); ISSUE(KA, VA, CIDX(i / IG + 2)); SCHED_FENCE();
      CONSUMEN((KI + i + IG), (VI + i + IG), IG); SCHED_FENCE();
      CONSUME(KB, VB); SCHED_FENCE(); ISSUE(KB, VB, CIDX(i / IG + 3)); SCHED_FENCE();
    }
    it0 = IL / IG;
  }
  const int it_w = STAG * w;
#pragma unroll 1
  for (int it = it0; it < it_w; it += 2) STEP2(it);
  const int c0 = (FS * h) >> 3;
  const int foff = FS * h - 8 * c0;
  u32x4 gv[FR], uv[FR];
  {
    const unsigned voff = (unsigned)(FR * r8) * (FF * 2) + 16 * min(c0 + c8, FF / 8 - 1);
    const char* gb = reinterpret_cast<const char*>(wg) + (size_t)(RW * w) * (FF * 2);
    const char* ub = reinterpret_cast<const char*>(wu) + (size_t)(RW * w) * (FF * 2);
#pragma unroll
    for (int i = 0; i < FR; ++i) { gv[i] = ldw(gb + (size_t)i * (FF * 2) + voff); uv[i] = ldw(ub + (size_t)i * (FF * 2) + voff); }
  }
  SCHED_FENCE();
  STEP2(it_w);
  {
    const u32x4 xa = *reinterpret_cast<const u32x4*>(xn16 + RW * w + FR * r8);
    float ag[8], au[8];
#pragma unroll
    for (int j = 0; j < 8; ++j) { ag[j] = 0.f; au[j] = 0.f; }
#pragma unroll
    for (int i = 0; i < FR; ++i) {
      const unsigned p = xa[i >> 1];
      const unsigned slo = (i & 1) ? (p >> 16) : (p & 0xffffu), shi = (i & 1) ? (p & 0xffff0000u) : (p << 16);
      axpy8(ag, gv[i], slo, shi);
      axpy8(au, uv[i], slo, shi);
    }
#pragma unroll
    for (int j = 0; j < 8; ++j) {
      ag[j] += dpp<ROR8>(ag[j]); ag[j] = xrow_sum(ag[j]);
      au[j] += dpp<ROR8>(au[j]); au[j] = xrow_sum(au[j]);
    }
    if (r8 == 0) {
      float* fp = &lds[L_FP + w * 128 + 8 * c8];
      *reinterpret_cast<f32x4*>(fp) = f32x4{ag[0], ag[1], ag[2], ag[3]};
      *reinterpret_cast<f32x4*>(fp + 4) = f32x4{ag[4], ag[5], ag[6], ag[7]};
      *reinterpret_cast<f32x4*>(fp + 64) = f32x4{au[0], au[1], au[2], au[3]};
      *reinterpret_cast<f32x4*>(fp + 68) = f32x4{au[4], au[5], au[6], au[7]};
    }
    MEM_FENCE();
    if (lane == 0) __hip_atomic_fetch_add(&ctr[0], 1u, __ATOMIC_RELAXED, __HIP_MEMORY_SCOPE_WORKGROUP);
    MEM_FENCE();
  }
  SCHED_FENCE();
  u32x4 wov[OR], wdv[DR];
  {
    const char* ob = reinterpret_cast<const char*>(wo) + (size_t)(h * DH + OR * w) * (DM * 2);
    const char* db = reinterpret_cast<const char*>(wd) + (size_t)(FS * h) * (DM * 2);
#pragma unroll
    for (int i = 0; i < OR; ++i) wov[i] = ldw(ob + i * (DM * 2) + loff);
#pragma unroll
    for (int i = 0; i < DR; ++i) wdv[i] = ldw(db + (size_t)min(w + NW * i, FS - 1) * (DM * 2) + loff);
  }
  SCHED_FENCE();
  {
    int it = it_w + 2;
#pragma unroll 1
    do { STEP2(it); it += 2; } while (it < NS - 2);
  }
  {
    while (__hip_atomic_load(&ctr[0], __ATOMIC_RELAXED, __HIP_MEMORY_SCOPE_WORKGROUP) < (unsigned)NW) __builtin_amdgcn_s_sleep(1);
    MEM_FENCE();
    float g = 0.f, u = 0.f;
    const int jr = min(w + NW * (lane & 7), FS - 1);
#pragma unroll
    for (int i = 0; i < NW; ++i) {
      g += lds[L_FP + i * 128 + foff + jr];
      u += lds[L_FP + i * 128 + 64 + foff + jr];
    }
    g = bfr(g); u = bfr(u);
    g = g > 0.f ? g : 0.f;
    const unsigned hb = __builtin_bit_cast(unsigned, bfr(g * u));
    float bb[8];
#pragma unroll
    for (int j = 0; j < 8; ++j) bb[j] = 0.f;
#pragma unroll
    for (int t = 0; t < DR; ++t) {
      unsigned s = __builtin_amdgcn_readlane(hb, t);
      if (w + NW * t >= FS) s = 0u;
      axpy8(bb, wdv[t], s >> 16, s);
    }
    *reinterpret_cast<f32x4*>(&lds[L_RB + w * 512 + 8 * lane]) = f32x4{bb[0], bb[1], bb[2], bb[3]};
    *reinterpret_cast<f32x4*>(&lds[L_RB + w * 512 + 8 * lane + 4]) = f32x4{bb[4], bb[5], bb[6], bb[7]};
#pragma unroll
    for (int i = 0; i < OR; i += 2) {
#pragma unroll
      for (int j = 0; j < 4; ++j) {
        const unsigned r0 = wov[i][j], r1 = wov[i + 1][j];
        wov[i][j] = __builtin_amdgcn_perm(r1, r0, 0x05040100u);
        wov[i + 1][j] = __builtin_amdgcn_perm(r1, r0, 0x07060302u);
      }
      asm volatile("" : "+v"(wov[i]), "+v"(wov[i + 1]));
    }
  }
  SCHED_FENCE();
  {
    const int n_w = w < QW ? NQ : NK;
    int it = NS - 2;
#pragma unroll 1
    do { STEP2(it); it += 2; } while (it < n_w - 2);
  }
  CONSUME(KA, VA);
  SCHED_FENCE();
  CONSUME(KB, VB);
#undef STEP2
#undef CIDX
#undef CONSUME
#undef CONSUMEN
#undef ISSUE

  {
    const float M = xrow_max(m);
    const float wt = __builtin_amdgcn_exp2f(m - M);
    l = xrow_sum(l * wt);
#pragma unroll
    for (int j = 0; j < 8; ++j) acc[j] = xrow_sum(acc[j] * wt);
    if (r == 0) {
      float* rec = &lds[L_REC + w * 132];
      *reinterpret_cast<f32x4*>(rec + 4 + 8 * c) = f32x4{acc[0], acc[1], acc[2], acc[3]};
      *reinterpret_cast<f32x4*>(rec + 8 + 8 * c) = f32x4{acc[4], acc[5], acc[6], acc[7]};
      if (c == 0) *reinterpret_cast<f32x2*>(rec) = f32x2{M, l};
    }
  }
  SCHED_FENCE();
  __syncthreads();
  SCHED_FENCE();
  {
    f32x2 ml[NW];
    float av[NW];
#pragma unroll
    for (int i = 0; i < NW; ++i) {
      ml[i] = *reinterpret_cast<const f32x2*>(&lds[L_REC + i * 132]);
      av[i] = lds[L_REC + i * 132 + 4 + OR * w + c];
    }
    float M = ml[0][0];
#pragma unroll
    for (int i = 1; i < NW; ++i) M = fmaxf(M, ml[i][0]);
    float L = 0.f, o = 0.f;
#pragma unroll
    for (int i = 0; i < NW; ++i) {
      const float wt = __builtin_amdgcn_exp2f(ml[i][0] - M);
      L = fmaf(wt, ml[i][1], L);
      o = fmaf(wt, av[i], o);
    }
    const unsigned ob = __builtin_bit_cast(unsigned, bfr(o / L));
    const unsigned nx = __builtin_amdgcn_mov_dpp(ob, 0x101  , 0xf, 0xf, true);
    const unsigned op = __builtin_amdgcn_perm(nx, ob, 0x07060302u);
    float a[8];
#pragma unroll
    for (int j = 0; j < 8; ++j) a[j] = 0.f;
#pragma unroll
    for (int i = 0; i < OR; i += 2) {
      const unsigned s = __builtin_amdgcn_readlane(op, i);
#pragma unroll
      for (int j = 0; j < 4; ++j) { a[2 * j] = dot2(wov[i][j], s, a[2 * j]); a[2 * j + 1] = dot2(wov[i + 1][j], s, a[2 * j + 1]); }
    }
    *reinterpret_cast<f32x4*>(&lds[L_RA + w * 512 + 8 * lane]) = f32x4{a[0], a[1], a[2], a[3]};
    *reinterpret_cast<f32x4*>(&lds[L_RA + w * 512 + 8 * lane + 4]) = f32x4{a[4], a[5], a[6], a[7]};
  }
  __syncthreads();
  {
    float sa = 0.f, sb = 0.f;
#pragma unroll
    for (int i = 0; i < NW; ++i) { sa += lds[L_RA + i * 512 + tid]; sb += lds[L_RB + i * 512 + tid]; }
    pa[(size_t)(b * NH + h) * DM + tid] = sa;
    pb[(size_t)(b * NH + h) * DM + tid] = sb;
  }
}

__global__ void __launch_bounds__(128) reduce_kernel(const uint16_t* __restrict__ x, const float* __restrict__ pa,
                                                     const float* __restrict__ pb, uint16_t* __restrict__ out) {
  const int idx = blockIdx.x * 128 + threadIdx.x;
  const int b = idx >> 9, n = idx & 511;
  float va[NH], vb[NH];
#pragma unroll
  for (int i = 0; i < NH; ++i) { va[i] = pa[(size_t)(b * NH + i) * DM + n]; vb[i] = pb[(size_t)(b * NH + i) * DM + n]; }
  float sa = 0.f, sb = 0.f;
#pragma unroll
  for (int i = 0; i < NH; ++i) { sa += va[i]; sb += vb[i]; }
  const float xv = __builtin_bit_cast(float, (unsigned)x[idx] << 16);
  const float o = bfr(bfr(xv + bfr(sa)) + bfr(sb));
  out[idx] = (uint16_t)(__builtin_bit_cast(unsigned, o) >> 16);
}

extern "C" void kernel_launch(void* const* d_in, const int* in_sizes, int n_in, void* d_out, int out_size,
                              void* d_ws, size_t ws_size, hipStream_t stream) {
  const uint16_t* x = (const uint16_t*)d_in[0];
  const uint16_t* kc = (const uint16_t*)d_in[1];
  const uint16_t* vc = (const uint16_t*)d_in[2];
  const uint16_t* wq = (const uint16_t*)d_in[3];
  const uint16_t* wo = (const uint16_t*)d_in[4];
  const uint16_t* wg = (const uint16_t*)d_in[6];
  const uint16_t* wu = (const uint16_t*)d_in[7];
  const uint16_t* wd = (const uint16_t*)d_in[8];
  float* pa = (float*)d_ws;
  float* pb = pa + NB * NH * DM;
  fused_decode_kernel<<<NB * NH, NT, 0, stream>>>(x, kc, vc, wq, wo, wg, wu, wd, pa, pb);
  reduce_kernel<<<NB * DM / 128, 128, 0, stream>>>(x, pa, pb, (uint16_t*)d_out);
}
```

```cpp
#define CFG_IB 96
#define CFG_D 8
#define CFG_THR 4
#include <hip/hip_runtime.h>
#include <hip/hip_bf16.h>
#include <stdint.h>

typedef unsigned u32x4 __attribute__((ext_vector_type(4)));
typedef unsigned u32x2 __attribute__((ext_vector_type(2)));
typedef float f32x4 __attribute__((ext_vector_type(4)));
typedef float f32x2 __attribute__((ext_vector_type(2)));
typedef __bf16 bf16x2 __attribute__((ext_vector_type(2)));

constexpr int NB = 8, DM = 512, NH = 32, SK = 4096, DH = 128, FF = 1376, FS = 43;
constexpr int NW = 8, NT = NW * 64;
#define CFG_CK 4
constexpr int CK = CFG_CK;
constexpr int CL = CK / 4;
constexpr int IB = CFG_IB;
constexpr int IL = IB / 4;
constexpr int NCHT = (SK - 4 * IB) / CK;
constexpr int NQ = NCHT / 8 + CFG_D / 2;
constexpr int NK = NCHT / 8 - CFG_D / 2;
#define CFG_NS 64
constexpr int NS = CFG_NS;
#define CFG_STAG (32 / CFG_CK)
constexpr int STAG = CFG_STAG;
constexpr int RW = DM / NW;
constexpr int QW = 4;
constexpr int QRW = DM / QW;
constexpr int QR = QRW / 4;
constexpr int FR = RW / 8;
constexpr int OR = DH / NW;
constexpr int DR = (FS + NW - 1) / NW;
constexpr float SCALE = 0.08838834764831845f;
constexpr float LOG2E = 1.4426950408889634f;
static_assert(STAG % 2 == 0 && STAG * (NW - 1) + 6 <= NS && NS % 2 == 0 && NS + 2 <= NK && NQ % 2 == 0 && NK % 2 == 0 && 4 * (NQ + NK) == NCHT && RW == 64, "config");

constexpr int L_XN = 0;
constexpr int L_QP = L_XN + NW * 32;
constexpr int L_QB = L_QP + NW * 128;
constexpr int L_FP = L_QB + 64;
constexpr int L_REC = L_FP + NW * 128;
constexpr int L_RA = L_REC + NW * 132;
constexpr int L_RB = L_RA + NW * 512;
constexpr int L_CT = L_RB + NW * 512;
constexpr int L_END = L_CT + 4;

__device__ __forceinline__ float bfr(float x) { return __bfloat162float(__float2bfloat16(x)); }
__device__ __forceinline__ float bf_lo(unsigned u) { return __builtin_bit_cast(float, u << 16); }
__device__ __forceinline__ float bf_hi(unsigned u) { return __builtin_bit_cast(float, u & 0xffff0000u); }
__device__ __forceinline__ float dot2(unsigned k, unsigned q, float acc) {
  return __builtin_amdgcn_fdot2_f32_bf16(__builtin_bit_cast(bf16x2, k), __builtin_bit_cast(bf16x2, q), acc, false);
}
__device__ __forceinline__ void axpy8(float (&acc)[8], const u32x4& row, unsigned slo, unsigned shi) {
#pragma unroll
  for (int j = 0; j < 4; ++j) { acc[2 * j] = dot2(row[j], slo, acc[2 * j]); acc[2 * j + 1] = dot2(row[j], shi, acc[2 * j + 1]); }
}
template <int CTRL> __device__ __forceinline__ float dpp(float x) {
  return __builtin_bit_cast(float, __builtin_amdgcn_mov_dpp(__builtin_bit_cast(int, x), CTRL, 0xf, 0xf, true));
}
constexpr int ROR1 = 0x121, ROR2 = 0x122, ROR4 = 0x124, ROR8 = 0x128;
__device__ __forceinline__ float row16_sum(float x) {
  x += dpp<ROR8>(x); x += dpp<ROR4>(x); x += dpp<ROR2>(x); x += dpp<ROR1>(x); return x;
}
__device__ __forceinline__ float xrow_sum(float x) {
  auto s = __builtin_amdgcn_permlane16_swap(__float_as_uint(x), __float_as_uint(x), false, false);
  x = __uint_as_float(s[0]) + __uint_as_float(s[1]);
  auto t = __builtin_amdgcn_permlane32_swap(__float_as_uint(x), __float_as_uint(x), false, false);
  return __uint_as_float(t[0]) + __uint_as_float(t[1]);
}
__device__ __forceinline__ float xrow_max(float x) {
  auto s = __builtin_amdgcn_permlane16_swap(__float_as_uint(x), __float_as_uint(x), false, false);
  x = fmaxf(__uint_as_float(s[0]), __uint_as_float(s[1]));
  auto t = __builtin_amdgcn_permlane32_swap(__float_as_uint(x), __float_as_uint(x), false, false);
  return fmaxf(__uint_as_float(t[0]), __uint_as_float(t[1]));
}
__device__ __forceinline__ u32x4 ldnt(const void* p) { return __builtin_nontemporal_load(reinterpret_cast<const u32x4*>(p)); }
__device__ __forceinline__ u32x4 ld16(const void* p) { return *reinterpret_cast<const u32x4*>(p); }
__device__ __forceinline__ u32x4 ldw(const void* p) { return ld16(p); }
#define SCHED_FENCE() __builtin_amdgcn_sched_barrier(0)
#define MEM_FENCE() asm volatile("" ::: "memory")

__global__ void __launch_bounds__(NT) fused_decode_kernel(
    const uint16_t* __restrict__ x, const uint16_t* __restrict__ kc, const uint16_t* __restrict__ vc,
    const uint16_t* __restrict__ wq, const uint16_t* __restrict__ wo, const uint16_t* __restrict__ wg,
    const uint16_t* __restrict__ wu, const uint16_t* __restrict__ wd, float* __restrict__ pa,
    float* __restrict__ pb) {
  __shared__ __attribute__((aligned(16))) float lds[L_END];
  const int h = blockIdx.x & (NH - 1), b = blockIdx.x >> 5;
  const int tid = threadIdx.x, lane = tid & 63, w = __builtin_amdgcn_readfirstlane(tid >> 6);
  const int r = lane >> 4, c = lane & 15, r8 = lane >> 3, c8 = lane & 7;

  const bool thr = CFG_THR > 0 && (__builtin_amdgcn_s_getreg((3 << 11) | 20) & 1) == 0;
  const char* kbase = reinterpret_cast<const char*>(kc) + (size_t)(b * NH + h) * (SK * DH * 2) + (size_t)(4 * IB) * (DH * 2);
  const char* vbase = reinterpret_cast<const char*>(vc) + (size_t)(b * NH + h) * (SK * DH * 2) + (size_t)(4 * IB) * (DH * 2);
  u32x4 KI[IL], VI[IL];
  const unsigned loff = lane * 16;
  constexpr size_t CSTRIDE = (size_t)CK * DH * 2;
  unsigned* ctr = reinterpret_cast<unsigned*>(&lds[L_CT]);
  u32x4 KA[CL], VA[CL], KB[CL], VB[CL];
#define ISSUE(KR, VR, ch) do { _Pragma("unroll") for (int i_ = 0; i_ < CL; ++i_) KR[i_] = ldnt(kbase + (size_t)(ch) * CSTRIDE + i_ * 1024 + loff); \
                               _Pragma("unroll") for (int i_ = 0; i_ < CL; ++i_) VR[i_] = ldnt(vbase + (size_t)(ch) * CSTRIDE + i_ * 1024 + loff); } while (0)
  uint16_t* xn16 = reinterpret_cast<uint16_t*>(&lds[L_XN]);
  if (tid == NT - 1) ctr[0] = 0u;
  if (w < QW) {
    const uint16_t* xb = x + b * DM;
    const u32x4 xall = ld16(xb + 8 * lane);
    const unsigned xm0 = xb[QRW * w + lane], xm1 = xb[QRW * w + 64 + lane];
    u32x4 wqv[QR];
    {
      const char* base = reinterpret_cast<const char*>(wq) + (size_t)(QRW * w) * (NH * DH * 2) + h * (DH * 2);
      const unsigned voff = (unsigned)(QR * r) * (NH * DH * 2) + 16 * c;
#pragma unroll
      for (int i = 0; i < QR; ++i) wqv[i] = ldw(base + (size_t)i * (NH * DH * 2) + voff);
    }
    SCHED_FENCE();
    __builtin_amdgcn_s_barrier();
    SCHED_FENCE();
    {
      float xs[8];
#pragma unroll
      for (int j = 0; j < 4; ++j) { xs[2 * j] = bf_lo(xall[j]); xs[2 * j + 1] = bf_hi(xall[j]); }
      float s = ((xs[0] + xs[1]) + (xs[2] + xs[3])) + ((xs[4] + xs[5]) + (xs[6] + xs[7]));
      s = xrow_sum(row16_sum(s));
      const float mean = s * (1.0f / DM);
      float v = 0.f;
#pragma unroll
      for (int j = 0; j < 8; ++j) { const float d = xs[j] - mean; v = fmaf(d, d, v); }
      v = xrow_sum(row16_sum(v));
      const float var_b = bfr(v * (1.0f / DM));
      const float mean_b = bfr(mean);
      const float den = bfr(sqrtf(bfr(var_b + 1e-5f)));
      const float xn0 = bfr(bfr(bf_lo(xm0) - mean_b) / den);
      const float xn1 = bfr(bfr(bf_lo(xm1) - mean_b) / den);
      xn16[QRW * w + lane] = (uint16_t)(__builtin_bit_cast(unsigned, xn0) >> 16);
      xn16[QRW * w + 64 + lane] = (uint16_t)(__builtin_bit_cast(unsigned, xn1) >> 16);
    }
    u32x4 xa[QR / 8];
#pragma unroll
    for (int i = 0; i < QR / 8; ++i) xa[i] = *reinterpret_cast<const u32x4*>(xn16 + QRW * w + QR * r + 8 * i);
    float acc[8];
#pragma unroll
    for (int j = 0; j < 8; ++j) acc[j] = 0.f;
#pragma unroll
    for (int i = 0; i < QR; ++i) {
      const unsigned p = xa[i >> 3][(i >> 1) & 3];
      axpy8(acc, wqv[i], (i & 1) ? (p >> 16) : (p & 0xffffu), (i & 1) ? (p & 0xffff0000u) : (p << 16));
    }
#pragma unroll
    for (int j = 0; j < 8; ++j) acc[j] = xrow_sum(acc[j]);
    if (r == 0) {
      *reinterpret_cast<f32x4*>(&lds[L_QP + w * 128 + 8 * c]) = f32x4{acc[0], acc[1], acc[2], acc[3]};
      *reinterpret_cast<f32x4*>(&lds[L_QP + w * 128 + 8 * c + 4]) = f32x4{acc[4], acc[5], acc[6], acc[7]};
    }
  } else {
    __builtin_amdgcn_s_barrier();
    SCHED_FENCE();
    const char* ki = reinterpret_cast<const char*>(kc) + (size_t)(b * NH + h) * (SK * DH * 2) + (size_t)((w - QW) * IB) * (DH * 2);
    const char* vi = reinterpret_cast<const char*>(vc) + (size_t)(b * NH + h) * (SK * DH * 2) + (size_t)((w - QW) * IB) * (DH * 2);
#pragma unroll
    for (int i = 0; i < IL; ++i) KI[i] = ldnt(ki + i * 1024 + loff);
#pragma unroll
    for (int i = 0; i < IL; ++i) VI[i] = ldnt(vi + i * 1024 + loff);
  }
  SCHED_FENCE();
  __syncthreads();
  if (tid < 128) {
    float s = 0.f;
#pragma unroll
    for (int i = 0; i < QW; ++i) s += lds[L_QP + i * 128 + tid];
    reinterpret_cast<uint16_t*>(&lds[L_QB])[tid] = (uint16_t)(__builtin_bit_cast(unsigned, bfr(s)) >> 16);
  }
  SCHED_FENCE();
  __syncthreads();
  ISSUE(KA, VA, w);
  ISSUE(KB, VB, NW + w);
  SCHED_FENCE();
  const u32x4 q = *reinterpret_cast<const u32x4*>(&lds[L_QB + 4 * c]);

  float m = -INFINITY, l = 0.f, acc[8];
#pragma unroll
  for (int j = 0; j < 8; ++j) acc[j] = 0.f;
#define CONSUME(KR, VR) CONSUMEN(KR, VR, CL)
#define CONSUMEN(KR, VR, CL) do { \
    float s_[CL]; float mn_ = m; \
    _Pragma("unroll") for (int i_ = 0; i_ < CL; ++i_) { \
      float d_ = dot2(KR[i_][0], q[0], 0.f); d_ = dot2(KR[i_][1], q[1], d_); d_ = dot2(KR[i_][2], q[2], d_); d_ = dot2(KR[i_][3], q[3], d_); \
      d_ = row16_sum(d_); s_[i_] = bfr(bfr(d_) * SCALE) * LOG2E; mn_ = fmaxf(mn_, s_[i_]); } \
    const float al_ = __builtin_amdgcn_exp2f(m - mn_); m = mn_; \
    float ps_ = 0.f; \
    _Pragma("unroll") for (int i_ = 0; i_ < CL; ++i_) { s_[i_] = __builtin_amdgcn_exp2f(s_[i_] - mn_); ps_ += s_[i_]; } \
    l = fmaf(l, al_, ps_); \
    _Pragma("unroll") for (int j_ = 0; j_ < 8; ++j_) acc[j_] *= al_; \
    _Pragma("unroll") for (int i_ = 0; i_ < CL; ++i_) { \
      _Pragma("unroll") for (int j_ = 0; j_ < 4; ++j_) { \
        acc[2 * j_] = fmaf(s_[i_], bf_lo(VR[i_][j_]), acc[2 * j_]); \
        acc[2 * j_ + 1] = fmaf(s_[i_], bf_hi(VR[i_][j_]), acc[2 * j_ + 1]); } } \
    asm volatile("" : "+v"(acc[0]), "+v"(acc[1]), "+v"(acc[2]), "+v"(acc[3]), "+v"(acc[4]), "+v"(acc[5]), "+v"(acc[6]), "+v"(acc[7]), "+v"(l), "+v"(m)); \
  } while (0)
#define CIDX(i) (((i) < NK ? NW * (i) : (NW - QW) * NK + QW * (i)) + w)
#define STEP2(it) do { \
    CONSUME(KA, VA); SCHED_FENCE(); if (thr) __builtin_amdgcn_s_sleep(CFG_THR); ISSUE(KA, VA, CIDX((it) + 2)); SCHED_FENCE(); \
    CONSUME(KB, VB); SCHED_FENCE(); ISSUE(KB, VB, CIDX((it) + 3)); SCHED_FENCE(); } while (0)

  if (w >= QW) {
#pragma unroll
    for (int i = 0; i < IL; i += 4) { CONSUMEN((KI + i), (VI + i), 4); SCHED_FENCE(); }
  }
  const int it_w = STAG * w;
#pragma unroll 1
  for (int it = 0; it < it_w; it += 2) STEP2(it);
  const int c0 = (FS * h) >> 3;
  const int foff = FS * h - 8 * c0;
  u32x4 gv[FR], uv[FR];
  {
    const unsigned voff = (unsigned)(FR * r8) * (FF * 2) + 16 * min(c0 + c8, FF / 8 - 1);
    const char* gb = reinterpret_cast<const char*>(wg) + (size_t)(RW * w) * (FF * 2);
    const char* ub = reinterpret_cast<const char*>(wu) + (size_t)(RW * w) * (FF * 2);
#pragma unroll
    for (int i = 0; i < FR; ++i) { gv[i] = ldw(gb + (size_t)i * (FF * 2) + voff); uv[i] = ldw(ub + (size_t)i * (FF * 2) + voff); }
  }
  SCHED_FENCE();
  STEP2(it_w);
  {
    const u32x4 xa = *reinterpret_cast<const u32x4*>(xn16 + RW * w + FR * r8);
    float ag[8], au[8];
#pragma unroll
    for (int j = 0; j < 8; ++j) { ag[j] = 0.f; au[j] = 0.f; }
#pragma unroll
    for (int i = 0; i < FR; ++i) {
      const unsigned p = xa[i >> 1];
      const unsigned slo = (i & 1) ? (p >> 16) : (p & 0xffffu), shi = (i & 1) ? (p & 0xffff0000u) : (p << 16);
      axpy8(ag, gv[i], slo, shi);
      axpy8(au, uv[i], slo, shi);
    }
#pragma unroll
    for (int j = 0; j < 8; ++j) {
      ag[j] += dpp<ROR8>(ag[j]); ag[j] = xrow_sum(ag[j]);
      au[j] += dpp<ROR8>(au[j]); au[j] = xrow_sum(au[j]);
    }
    if (r8 == 0) {
      float* fp = &lds[L_FP + w * 128 + 8 * c8];
      *reinterpret_cast<f32x4*>(fp) = f32x4{ag[0], ag[1], ag[2], ag[3]};
      *reinterpret_cast<f32x4*>(fp + 4) = f32x4{ag[4], ag[5], ag[6], ag[7]};
      *reinterpret_cast<f32x4*>(fp + 64) = f32x4{au[0], au[1], au[2], au[3]};
      *reinterpret_cast<f32x4*>(fp + 68) = f32x4{au[4], au[5], au[6], au[7]};
    }
    MEM_FENCE();
    if (lane == 0) __hip_atomic_fetch_add(&ctr[0], 1u, __ATOMIC_RELAXED, __HIP_MEMORY_SCOPE_WORKGROUP);
    MEM_FENCE();
  }
  SCHED_FENCE();
  u32x4 wov[OR], wdv[DR];
  {
    const char* ob = reinterpret_cast<const char*>(wo) + (size_t)(h * DH + OR * w) * (DM * 2);
    const char* db = reinterpret_cast<const char*>(wd) + (size_t)(FS * h) * (DM * 2);
#pragma unroll
    for (int i = 0; i < OR; ++i) wov[i] = ldw(ob + i * (DM * 2) + loff);
#pragma unroll
    for (int i = 0; i < DR; ++i) wdv[i] = ldw(db + (size_t)min(w + NW * i, FS - 1) * (DM * 2) + loff);
  }
  SCHED_FENCE();
  {
    int it = it_w + 2;
#pragma unroll 1
    do { STEP2(it); it += 2; } while (it < NS - 2);
  }
  {
    while (__hip_atomic_load(&ctr[0], __ATOMIC_RELAXED, __HIP_MEMORY_SCOPE_WORKGROUP) < (unsigned)NW) __builtin_amdgcn_s_sleep(1);
    MEM_FENCE();
    float g = 0.f, u = 0.f;
    const int jr = min(w + NW * (lane & 7), FS - 1);
#pragma unroll
    for (int i = 0; i < NW; ++i) {
      g += lds[L_FP + i * 128 + foff + jr];
      u += lds[L_FP + i * 128 + 64 + foff + jr];
    }
    g = bfr(g); u = bfr(u);
    g = g > 0.f ? g : 0.f;
    const unsigned hb = __builtin_bit_cast(unsigned, bfr(g * u));
    float bb[8];
#pragma unroll
    for (int j = 0; j < 8; ++j) bb[j] = 0.f;
#pragma unroll
    for (int t = 0; t < DR; ++t) {
      unsigned s = __builtin_amdgcn_readlane(hb, t);
      if (w + NW * t >= FS) s = 0u;
      axpy8(bb, wdv[t], s >> 16, s);
    }
    *reinterpret_cast<f32x4*>(&lds[L_RB + w * 512 + 8 * lane]) = f32x4{bb[0], bb[1], bb[2], bb[3]};
    *reinterpret_cast<f32x4*>(&lds[L_RB + w * 512 + 8 * lane + 4]) = f32x4{bb[4], bb[5], bb[6], bb[7]};
#pragma unroll
    for (int i = 0; i < OR; i += 2) {
#pragma unroll
      for (int j = 0; j < 4; ++j) {
        const unsigned r0 = wov[i][j], r1 = wov[i + 1][j];
        wov[i][j] = __builtin_amdgcn_perm(r1, r0, 0x05040100u);
        wov[i + 1][j] = __builtin_amdgcn_perm(r1, r0, 0x07060302u);
      }
      asm volatile("" : "+v"(wov[i]), "+v"(wov[i + 1]));
    }
  }
  SCHED_FENCE();
  {
    const int n_w = w < QW ? NQ : NK;
    int it = NS - 2;
#pragma unroll 1
    do { STEP2(it); it += 2; } while (it < n_w - 2);
  }
  CONSUME(KA, VA);
  SCHED_FENCE();
  CONSUME(KB, VB);
#undef STEP2
#undef CIDX
#undef CONSUME
#undef CONSUMEN
#undef ISSUE

  {
    const float M = xrow_max(m);
    const float wt = __builtin_amdgcn_exp2f(m - M);
    l = xrow_sum(l * wt);
#pragma unroll
    for (int j = 0; j < 8; ++j) acc[j] = xrow_sum(acc[j] * wt);
    if (r == 0) {
      float* rec = &lds[L_REC + w * 132];
      *reinterpret_cast<f32x4*>(rec + 4 + 8 * c) = f32x4{acc[0], acc[1], acc[2], acc[3]};
      *reinterpret_cast<f32x4*>(rec + 8 + 8 * c) = f32x4{acc[4], acc[5], acc[6], acc[7]};
      if (c == 0) *reinterpret_cast<f32x2*>(rec) = f32x2{M, l};
    }
  }
  SCHED_FENCE();
  __syncthreads();
  SCHED_FENCE();
  {
    f32x2 ml[NW];
    float av[NW];
#pragma unroll
    for (int i = 0; i < NW; ++i) {
      ml[i] = *reinterpret_cast<const f32x2*>(&lds[L_REC + i * 132]);
      av[i] = lds[L_REC + i * 132 + 4 + OR * w + c];
    }
    float M = ml[0][0];
#pragma unroll
    for (int i = 1; i < NW; ++i) M = fmaxf(M, ml[i][0]);
    float L = 0.f, o = 0.f;
#pragma unroll
    for (int i = 0; i < NW; ++i) {
      const float wt = __builtin_amdgcn_exp2f(ml[i][0] - M);
      L = fmaf(wt, ml[i][1], L);
      o = fmaf(wt, av[i], o);
    }
    const unsigned ob = __builtin_bit_cast(unsigned, bfr(o / L));
    const unsigned nx = __builtin_amdgcn_mov_dpp(ob, 0x101  , 0xf, 0xf, true);
    const unsigned op = __builtin_amdgcn_perm(nx, ob, 0x07060302u);
    float a[8];
#pragma unroll
    for (int j = 0; j < 8; ++j) a[j] = 0.f;
#pragma unroll
    for (int i = 0; i < OR; i += 2) {
      const unsigned s = __builtin_amdgcn_readlane(op, i);
#pragma unroll
      for (int j = 0; j < 4; ++j) { a[2 * j] = dot2(wov[i][j], s, a[2 * j]); a[2 * j + 1] = dot2(wov[i + 1][j], s, a[2 * j + 1]); }
    }
    *reinterpret_cast<f32x4*>(&lds[L_RA + w * 512 + 8 * lane]) = f32x4{a[0], a[1], a[2], a[3]};
    *reinterpret_cast<f32x4*>(&lds[L_RA + w * 512 + 8 * lane + 4]) = f32x4{a[4], a[5], a[6], a[7]};
  }
  __syncthreads();
  {
    float sa = 0.f, sb = 0.f;
#pragma unroll
    for (int i = 0; i < NW; ++i) { sa += lds[L_RA + i * 512 + tid]; sb += lds[L_RB + i * 512 + tid]; }
    pa[(size_t)(b * NH + h) * DM + tid] = sa;
    pb[(size_t)(b * NH + h) * DM + tid] = sb;
  }
}

__global__ void __launch_bounds__(128) reduce_kernel(const uint16_t* __restrict__ x, const float* __restrict__ pa,
                                                     const float* __restrict__ pb, uint16_t* __restrict__ out) {
  const int idx = blockIdx.x * 128 + threadIdx.x;
  const int b = idx >> 9, n = idx & 511;
  float va[NH], vb[NH];
#pragma unroll
  for (int i = 0; i < NH; ++i) { va[i] = pa[(size_t)(b * NH + i) * DM + n]; vb[i] = pb[(size_t)(b * NH + i) * DM + n]; }
  float sa = 0.f, sb = 0.f;
#pragma unroll
  for (int i = 0; i < NH; ++i) { sa += va[i]; sb += vb[i]; }
  const float xv = __builtin_bit_cast(float, (unsigned)x[idx] << 16);
  const float o = bfr(bfr(xv + bfr(sa)) + bfr(sb));
  out[idx] = (uint16_t)(__builtin_bit_cast(unsigned, o) >> 16);
}

extern "C" void kernel_launch(void* const* d_in, const int* in_sizes, int n_in, void* d_out, int out_size,
                              void* d_ws, size_t ws_size, hipStream_t stream) {
  const uint16_t* x = (const uint16_t*)d_in[0];
  const uint16_t* kc = (const uint16_t*)d_in[1];
  const uint16_t* vc = (const uint16_t*)d_in[2];
  const uint16_t* wq = (const uint16_t*)d_in[3];
  const uint16_t* wo = (const uint16_t*)d_in[4];
  const uint16_t* wg = (const uint16_t*)d_in[6];
  const uint16_t* wu = (const uint16_t*)d_in[7];
  const uint16_t* wd = (const uint16_t*)d_in[8];
  float* pa = (float*)d_ws;
  float* pb = pa + NB * NH * DM;
  fused_decode_kernel<<<NB * NH, NT, 0, stream>>>(x, kc, vc, wq, wo, wg, wu, wd, pa, pb);
  reduce_kernel<<<NB * DM / 128, 128, 0, stream>>>(x, pa, pb, (uint16_t*)d_out);
}
```

```cpp
#define CFG_IB 96
#define CFG_D 8
#define CFG_THR 0
#define CFG_NS 66
#include <hip/hip_runtime.h>
#include <hip/hip_bf16.h>
#include <stdint.h>

typedef unsigned u32x4 __attribute__((ext_vector_type(4)));
typedef unsigned u32x2 __attribute__((ext_vector_type(2)));
typedef float f32x4 __attribute__((ext_vector_type(4)));
typedef float f32x2 __attribute__((ext_vector_type(2)));
typedef __bf16 bf16x2 __attribute__((ext_vector_type(2)));

constexpr int NB = 8, DM = 512, NH = 32, SK = 4096, DH = 128, FF = 1376, FS = 43;
constexpr int NW = 8, NT = NW * 64;
#define CFG_CK 4
constexpr int CK = CFG_CK;
constexpr int CL = CK / 4;
constexpr int IB = CFG_IB;
constexpr int IL = IB / 4;
constexpr int NCHT = (SK - 4 * IB) / CK;
constexpr int NQ = NCHT / 8 + CFG_D / 2;
constexpr int NK = NCHT / 8 - CFG_D / 2;
constexpr int NS = CFG_NS;
#define CFG_STAG (32 / CFG_CK)
constexpr int STAG = CFG_STAG;
constexpr int RW = DM / NW;
constexpr int QW = 4;
constexpr int QRW = DM / QW;
constexpr int QR = QRW / 4;
constexpr int FR = RW / 4;
constexpr int OR = DH / NW;
constexpr int DR = (2 * FS + NW - 1) / NW;
constexpr float SCALE = 0.08838834764831845f;
constexpr float LOG2E = 1.4426950408889634f;
static_assert(STAG % 2 == 0 && STAG * (NW - 1) + 8 <= NS && NS % 2 == 0 && NS + 2 <= NK && NQ % 2 == 0 && NK % 2 == 0 && 4 * (NQ + NK) == NCHT && RW == 64, "config");

constexpr int L_XN = 0;
constexpr int L_QP = L_XN + NW * 32;
constexpr int L_QB = L_QP + NW * 128;
constexpr int L_FP = L_QB + 64;
constexpr int L_REC = L_FP + NW * 256;
constexpr int L_RA = L_REC + NW * 132;
constexpr int L_RB = L_RA + NW * 512;
constexpr int L_CT = L_RB + NW * 512;
constexpr int L_END = L_CT + 4;

__device__ __forceinline__ float bfr(float x) { return __bfloat162float(__float2bfloat16(x)); }
__device__ __forceinline__ float bf_lo(unsigned u) { return __builtin_bit_cast(float, u << 16); }
__device__ __forceinline__ float bf_hi(unsigned u) { return __builtin_bit_cast(float, u & 0xffff0000u); }
__device__ __forceinline__ float dot2(unsigned k, unsigned q, float acc) {
  return __builtin_amdgcn_fdot2_f32_bf16(__builtin_bit_cast(bf16x2, k), __builtin_bit_cast(bf16x2, q), acc, false);
}
__device__ __forceinline__ void axpy8(float (&acc)[8], const u32x4& row, unsigned slo, unsigned shi) {
#pragma unroll
  for (int j = 0; j < 4; ++j) { acc[2 * j] = dot2(row[j], slo, acc[2 * j]); acc[2 * j + 1] = dot2(row[j], shi, acc[2 * j + 1]); }
}
template <int CTRL> __device__ __forceinline__ float dpp(float x) {
  return __builtin_bit_cast(float, __builtin_amdgcn_mov_dpp(__builtin_bit_cast(int, x), CTRL, 0xf, 0xf, true));
}
constexpr int ROR1 = 0x121, ROR2 = 0x122, ROR4 = 0x124, ROR8 = 0x128;
__device__ __forceinline__ float row16_sum(float x) {
  x += dpp<ROR8>(x); x += dpp<ROR4>(x); x += dpp<ROR2>(x); x += dpp<ROR1>(x); return x;
}
__device__ __forceinline__ float xrow_sum(float x) {
  auto s = __builtin_amdgcn_permlane16_swap(__float_as_uint(x), __float_as_uint(x), false, false);
  x = __uint_as_float(s[0]) + __uint_as_float(s[1]);
  auto t = __builtin_amdgcn_permlane32_swap(__float_as_uint(x), __float_as_uint(x), false, false);
  return __uint_as_float(t[0]) + __uint_as_float(t[1]);
}
__device__ __forceinline__ float xrow_max(float x) {
  auto s = __builtin_amdgcn_permlane16_swap(__float_as_uint(x), __float_as_uint(x), false, false);
  x = fmaxf(__uint_as_float(s[0]), __uint_as_float(s[1]));
  auto t = __builtin_amdgcn_permlane32_swap(__float_as_uint(x), __float_as_uint(x), false, false);
  return fmaxf(__uint_as_float(t[0]), __uint_as_float(t[1]));
}
__device__ __forceinline__ u32x4 ldnt(const void* p) { return __builtin_nontemporal_load(reinterpret_cast<const u32x4*>(p)); }
__device__ __forceinline__ u32x4 ld16(const void* p) { return *reinterpret_cast<const u32x4*>(p); }
__device__ __forceinline__ u32x4 ldw(const void* p) { return ld16(p); }
#define SCHED_FENCE() __builtin_amdgcn_sched_barrier(0)
#define MEM_FENCE() asm volatile("" ::: "memory")

__global__ void __launch_bounds__(NT) fused_decode_kernel(
    const uint16_t* __restrict__ x, const uint16_t* __restrict__ kc, const uint16_t* __restrict__ vc,
    const uint16_t* __restrict__ wq, const uint16_t* __restrict__ wo, const uint16_t* __restrict__ wg,
    const uint16_t* __restrict__ wu, const uint16_t* __restrict__ wd, float* __restrict__ pa,
    float* __restrict__ pb) {
  __shared__ __attribute__((aligned(16))) float lds[L_END];
  const int h = blockIdx.x & (NH - 1), b = blockIdx.x >> 5;
  const int tid = threadIdx.x, lane = tid & 63, w = __builtin_amdgcn_readfirstlane(tid >> 6);
  const int r = lane >> 4, c = lane & 15, r8 = lane >> 3, c8 = lane & 7;

  const bool thr = CFG_THR > 0 && (__builtin_amdgcn_s_getreg((3 << 11) | 20) & 1) == 0;
  const char* kbase = reinterpret_cast<const char*>(kc) + (size_t)(b * NH + h) * (SK * DH * 2) + (size_t)(4 * IB) * (DH * 2);
  const char* vbase = reinterpret_cast<const char*>(vc) + (size_t)(b * NH + h) * (SK * DH * 2) + (size_t)(4 * IB) * (DH * 2);
  u32x4 KI[IL], VI[IL];
  const unsigned loff = lane * 16;
  constexpr size_t CSTRIDE = (size_t)CK * DH * 2;
  unsigned* ctr = reinterpret_cast<unsigned*>(&lds[L_CT]);
  u32x4 KA[CL], VA[CL], KB[CL], VB[CL];
#define ISSUE(KR, VR, ch) do { _Pragma("unroll") for (int i_ = 0; i_ < CL; ++i_) KR[i_] = ldnt(kbase + (size_t)(ch) * CSTRIDE + i_ * 1024 + loff); \
                               _Pragma("unroll") for (int i_ = 0; i_ < CL; ++i_) VR[i_] = ldnt(vbase + (size_t)(ch) * CSTRIDE + i_ * 1024 + loff); } while (0)
  uint16_t* xn16 = reinterpret_cast<uint16_t*>(&lds[L_XN]);
  if (tid == NT - 1) ctr[0] = 0u;
  if (w < QW) {
    const uint16_t* xb = x + b * DM;
    const u32x4 xall = ld16(xb + 8 * lane);
    const unsigned xm0 = xb[QRW * w + lane], xm1 = xb[QRW * w + 64 + lane];
    u32x4 wqv[QR];
    {
      const char* base = reinterpret_cast<const char*>(wq) + (size_t)(QRW * w) * (NH * DH * 2) + h * (DH * 2);
      const unsigned voff = (unsigned)(QR * r) * (NH * DH * 2) + 16 * c;
#pragma unroll
      for (int i = 0; i < QR; ++i) wqv[i] = ldw(base + (size_t)i * (NH * DH * 2) + voff);
    }
    SCHED_FENCE();
    __builtin_amdgcn_s_barrier();
    SCHED_FENCE();
    {
      float xs[8];
#pragma unroll
      for (int j = 0; j < 4; ++j) { xs[2 * j] = bf_lo(xall[j]); xs[2 * j + 1] = bf_hi(xall[j]); }
      float s = ((xs[0] + xs[1]) + (xs[2] + xs[3])) + ((xs[4] + xs[5]) + (xs[6] + xs[7]));
      s = xrow_sum(row16_sum(s));
      const float mean = s * (1.0f / DM);
      float v = 0.f;
#pragma unroll
      for (int j = 0; j < 8; ++j) { const float d = xs[j] - mean; v = fmaf(d, d, v); }
      v = xrow_sum(row16_sum(v));
      const float var_b = bfr(v * (1.0f / DM));
      const float mean_b = bfr(mean);
      const float den = bfr(sqrtf(bfr(var_b + 1e-5f)));
      const float xn0 = bfr(bfr(bf_lo(xm0) - mean_b) / den);
      const float xn1 = bfr(bfr(bf_lo(xm1) - mean_b) / den);
      xn16[QRW * w + lane] = (uint16_t)(__builtin_bit_cast(unsigned, xn0) >> 16);
      xn16[QRW * w + 64 + lane] = (uint16_t)(__builtin_bit_cast(unsigned, xn1) >> 16);
    }
    u32x4 xa[QR / 8];
#pragma unroll
    for (int i = 0; i < QR / 8; ++i) xa[i] = *reinterpret_cast<const u32x4*>(xn16 + QRW * w + QR * r + 8 * i);
    float acc[8];
#pragma unroll
    for (int j = 0; j < 8; ++j) acc[j] = 0.f;
#pragma unroll
    for (int i = 0; i < QR; ++i) {
      const unsigned p = xa[i >> 3][(i >> 1) & 3];
      axpy8(acc, wqv[i], (i & 1) ? (p >> 16) : (p & 0xffffu), (i & 1) ? (p & 0xffff0000u) : (p << 16));
    }
#pragma unroll
    for (int j = 0; j < 8; ++j) acc[j] = xrow_sum(acc[j]);
    if (r == 0) {
      *reinterpret_cast<f32x4*>(&lds[L_QP + w * 128 + 8 * c]) = f32x4{acc[0], acc[1], acc[2], acc[3]};
      *reinterpret_cast<f32x4*>(&lds[L_QP + w * 128 + 8 * c + 4]) = f32x4{acc[4], acc[5], acc[6], acc[7]};
    }
  } else {
    __builtin_amdgcn_s_barrier();
    SCHED_FENCE();
    const char* ki = reinterpret_cast<const char*>(kc) + (size_t)(b * NH + h) * (SK * DH * 2) + (size_t)((w - QW) * IB) * (DH * 2);
    const char* vi = reinterpret_cast<const char*>(vc) + (size_t)(b * NH + h) * (SK * DH * 2) + (size_t)((w - QW) * IB) * (DH * 2);
#pragma unroll
    for (int i = 0; i < IL; ++i) KI[i] = ldnt(ki + i * 1024 + loff);
#pragma unroll
    for (int i = 0; i < IL; ++i) VI[i] = ldnt(vi + i * 1024 + loff);
  }
  SCHED_FENCE();
  __syncthreads();
  if (tid < 128) {
    float s = 0.f;
#pragma unroll
    for (int i = 0; i < QW; ++i) s += lds[L_QP + i * 128 + tid];
    reinterpret_cast<uint16_t*>(&lds[L_QB])[tid] = (uint16_t)(__builtin_bit_cast(unsigned, bfr(s)) >> 16);
  }
  SCHED_FENCE();
  __syncthreads();
  ISSUE(KA, VA, w);
  ISSUE(KB, VB, NW + w);
  SCHED_FENCE();
  const u32x4 q = *reinterpret_cast<const u32x4*>(&lds[L_QB + 4 * c]);

  float m = -INFINITY, l = 0.f, acc[8];
#pragma unroll
  for (int j = 0; j < 8; ++j) acc[j] = 0.f;
#define CONSUME(KR, VR) CONSUMEN(KR, VR, CL)
#define CONSUMEN(KR, VR, CL) do { \
    float s_[CL]; float mn_ = m; \
    _Pragma("unroll") for (int i_ = 0; i_ < CL; ++i_) { \
      float d_ = dot2(KR[i_][0], q[0], 0.f); d_ = dot2(KR[i_][1], q[1], d_); d_ = dot2(KR[i_][2], q[2], d_); d_ = dot2(KR[i_][3], q[3], d_); \
      d_ = row16_sum(d_); s_[i_] = bfr(bfr(d_) * SCALE) * LOG2E; mn_ = fmaxf(mn_, s_[i_]); } \
    const float al_ = __builtin_amdgcn_exp2f(m - mn_); m = mn_; \
    float ps_ = 0.f; \
    _Pragma("unroll") for (int i_ = 0; i_ < CL; ++i_) { s_[i_] = __builtin_amdgcn_exp2f(s_[i_] - mn_); ps_ += s_[i_]; } \
    l = fmaf(l, al_, ps_); \
    _Pragma("unroll") for (int j_ = 0; j_ < 8; ++j_) acc[j_] *= al_; \
    _Pragma("unroll") for (int i_ = 0; i_ < CL; ++i_) { \
      _Pragma("unroll") for (int j_ = 0; j_ < 4; ++j_) { \
        acc[2 * j_] = fmaf(s_[i_], bf_lo(VR[i_][j_]), acc[2 * j_]); \
        acc[2 * j_ + 1] = fmaf(s_[i_], bf_hi(VR[i_][j_]), acc[2 * j_ + 1]); } } \
    asm volatile("" : "+v"(acc[0]), "+v"(acc[1]), "+v"(acc[2]), "+v"(acc[3]), "+v"(acc[4]), "+v"(acc[5]), "+v"(acc[6]), "+v"(acc[7]), "+v"(l), "+v"(m)); \
  } while (0)
#define CIDX(i) (((i) < NK ? NW * (i) : (NW - QW) * NK + QW * (i)) + w)
#define STEP2(it) do { \
    CONSUME(KA, VA); SCHED_FENCE(); if (thr) __builtin_amdgcn_s_sleep(CFG_THR); ISSUE(KA, VA, CIDX((it) + 2)); SCHED_FENCE(); \
    CONSUME(KB, VB); SCHED_FENCE(); ISSUE(KB, VB, CIDX((it) + 3)); SCHED_FENCE(); } while (0)

  if (w >= QW) {
#pragma unroll
    for (int i = 0; i < IL; i += 4) { CONSUMEN((KI + i), (VI + i), 4); SCHED_FENCE(); }
  }
  const int it_w = STAG * w;
#pragma unroll 1
  for (int it = 0; it < it_w; it += 2) STEP2(it);
  const bool ffn = (h & 1) == 0;
  const int c0 = (FS * h) >> 3;
  const int foff = FS * h - 8 * c0;
  u32x4 wov[OR], wdv[DR];
  const char* ob = reinterpret_cast<const char*>(wo) + (size_t)(h * DH + OR * w) * (DM * 2);
  if (ffn) {
    u32x4 gv[FR], uv[FR];
    {
      const unsigned voff = (unsigned)(FR * r) * (FF * 2) + 16 * min(c0 + min(c, 11), FF / 8 - 1);
      const char* gb = reinterpret_cast<const char*>(wg) + (size_t)(RW * w) * (FF * 2);
      const char* ub = reinterpret_cast<const char*>(wu) + (size_t)(RW * w) * (FF * 2);
#pragma unroll
      for (int i = 0; i < FR; ++i) { gv[i] = ldw(gb + (size_t)i * (FF * 2) + voff); uv[i] = ldw(ub + (size_t)i * (FF * 2) + voff); }
    }
    SCHED_FENCE();
    STEP2(it_w);
    {
      u32x4 xa[FR / 8];
#pragma unroll
      for (int i = 0; i < FR / 8; ++i) xa[i] = *reinterpret_cast<const u32x4*>(xn16 + RW * w + FR * r + 8 * i);
      float ag[8], au[8];
#pragma unroll
      for (int j = 0; j < 8; ++j) { ag[j] = 0.f; au[j] = 0.f; }
#pragma unroll
      for (int i = 0; i < FR; ++i) {
        const unsigned p = xa[i >> 3][(i >> 1) & 3];
        const unsigned slo = (i & 1) ? (p >> 16) : (p & 0xffffu), shi = (i & 1) ? (p & 0xffff0000u) : (p << 16);
        axpy8(ag, gv[i], slo, shi);
        axpy8(au, uv[i], slo, shi);
      }
#pragma unroll
      for (int j = 0; j < 8; ++j) { ag[j] = xrow_sum(ag[j]); au[j] = xrow_sum(au[j]); }
      if (r == 0) {
        float* fp = &lds[L_FP + w * 256 + 8 * c];
        *reinterpret_cast<f32x4*>(fp) = f32x4{ag[0], ag[1], ag[2], ag[3]};
        *reinterpret_cast<f32x4*>(fp + 4) = f32x4{ag[4], ag[5], ag[6], ag[7]};
        *reinterpret_cast<f32x4*>(fp + 128) = f32x4{au[0], au[1], au[2], au[3]};
        *reinterpret_cast<f32x4*>(fp + 132) = f32x4{au[4], au[5], au[6], au[7]};
      }
      MEM_FENCE();
      if (lane == 0) __hip_atomic_fetch_add(&ctr[0], 1u, __ATOMIC_RELAXED, __HIP_MEMORY_SCOPE_WORKGROUP);
      MEM_FENCE();
    }
    SCHED_FENCE();
    const char* db = reinterpret_cast<const char*>(wd) + (size_t)(FS * h) * (DM * 2);
#pragma unroll
    for (int i = 0; i < OR; ++i) wov[i] = ldw(ob + i * (DM * 2) + loff);
#pragma unroll
    for (int i = 0; i < DR; ++i) wdv[i] = ldw(db + (size_t)min(w + NW * i, 2 * FS - 1) * (DM * 2) + loff);
    SCHED_FENCE();
    STEP2(it_w + 2);
  } else {
    STEP2(it_w);
#pragma unroll
    for (int i = 0; i < OR; ++i) wov[i] = ldw(ob + i * (DM * 2) + loff);
#pragma unroll
    for (int i = 0; i < DR; ++i) wdv[i] = u32x4{0u, 0u, 0u, 0u};
    SCHED_FENCE();
    STEP2(it_w + 2);
  }
  {
    int it = it_w + 4;
#pragma unroll 1
    do { STEP2(it); it += 2; } while (it < NS - 2);
  }
  if (ffn) {
    while (__hip_atomic_load(&ctr[0], __ATOMIC_RELAXED, __HIP_MEMORY_SCOPE_WORKGROUP) < (unsigned)NW) __builtin_amdgcn_s_sleep(1);
    MEM_FENCE();
    float g = 0.f, u = 0.f;
    const int jr = min(w + NW * c, 2 * FS - 1);
#pragma unroll
    for (int i = 0; i < NW; ++i) {
      g += lds[L_FP + i * 256 + foff + jr];
      u += lds[L_FP + i * 256 + 128 + foff + jr];
    }
    g = bfr(g); u = bfr(u);
    g = g > 0.f ? g : 0.f;
    const unsigned hb = __builtin_bit_cast(unsigned, bfr(g * u));
    float bb[8];
#pragma unroll
    for (int j = 0; j < 8; ++j) bb[j] = 0.f;
#pragma unroll
    for (int t = 0; t < DR; ++t) {
      unsigned s = __builtin_amdgcn_readlane(hb, t);
      if (w + NW * t >= 2 * FS) s = 0u;
      axpy8(bb, wdv[t], s >> 16, s);
    }
    *reinterpret_cast<f32x4*>(&lds[L_RB + w * 512 + 8 * lane]) = f32x4{bb[0], bb[1], bb[2], bb[3]};
    *reinterpret_cast<f32x4*>(&lds[L_RB + w * 512 + 8 * lane + 4]) = f32x4{bb[4], bb[5], bb[6], bb[7]};
  }
  SCHED_FENCE();
  {
#pragma unroll
    for (int i = 0; i < OR; i += 2) {
#pragma unroll
      for (int j = 0; j < 4; ++j) {
        const unsigned r0 = wov[i][j], r1 = wov[i + 1][j];
        wov[i][j] = __builtin_amdgcn_perm(r1, r0, 0x05040100u);
        wov[i + 1][j] = __builtin_amdgcn_perm(r1, r0, 0x07060302u);
      }
      asm volatile("" : "+v"(wov[i]), "+v"(wov[i + 1]));
    }
  }
  SCHED_FENCE();
  {
    const int n_w = w < QW ? NQ : NK;
    int it = NS - 2;
#pragma unroll 1
    do { STEP2(it); it += 2; } while (it < n_w - 2);
  }
  CONSUME(KA, VA);
  SCHED_FENCE();
  CONSUME(KB, VB);
#undef STEP2
#undef CIDX
#undef CONSUME
#undef CONSUMEN
#undef ISSUE

  {
    const float M = xrow_max(m);
    const float wt = __builtin_amdgcn_exp2f(m - M);
    l = xrow_sum(l * wt);
#pragma unroll
    for (int j = 0; j < 8; ++j) acc[j] = xrow_sum(acc[j] * wt);
    if (r == 0) {
      float* rec = &lds[L_REC + w * 132];
      *reinterpret_cast<f32x4*>(rec + 4 + 8 * c) = f32x4{acc[0], acc[1], acc[2], acc[3]};
      *reinterpret_cast<f32x4*>(rec + 8 + 8 * c) = f32x4{acc[4], acc[5], acc[6], acc[7]};
      if (c == 0) *reinterpret_cast<f32x2*>(rec) = f32x2{M, l};
    }
  }
  SCHED_FENCE();
  __syncthreads();
  SCHED_FENCE();
  {
    f32x2 ml[NW];
    float av[NW];
#pragma unroll
    for (int i = 0; i < NW; ++i) {
      ml[i] = *reinterpret_cast<const f32x2*>(&lds[L_REC + i * 132]);
      av[i] = lds[L_REC + i * 132 + 4 + OR * w + c];
    }
    float M = ml[0][0];
#pragma unroll
    for (int i = 1; i < NW; ++i) M = fmaxf(M, ml[i][0]);
    float L = 0.f, o = 0.f;
#pragma unroll
    for (int i = 0; i < NW; ++i) {
      const float wt = __builtin_amdgcn_exp2f(ml[i][0] - M);
      L = fmaf(wt, ml[i][1], L);
      o = fmaf(wt, av[i], o);
    }
    const unsigned ob = __builtin_bit_cast(unsigned, bfr(o / L));
    const unsigned nx = __builtin_amdgcn_mov_dpp(ob, 0x101  , 0xf, 0xf, true);
    const unsigned op = __builtin_amdgcn_perm(nx, ob, 0x07060302u);
    float a[8];
#pragma unroll
    for (int j = 0; j < 8; ++j) a[j] = 0.f;
#pragma unroll
    for (int i = 0; i < OR; i += 2) {
      const unsigned s = __builtin_amdgcn_readlane(op, i);
#pragma unroll
      for (int j = 0; j < 4; ++j) { a[2 * j] = dot2(wov[i][j], s, a[2 * j]); a[2 * j + 1] = dot2(wov[i + 1][j], s, a[2 * j + 1]); }
    }
    *reinterpret_cast<f32x4*>(&lds[L_RA + w * 512 + 8 * lane]) = f32x4{a[0], a[1], a[2], a[3]};
    *reinterpret_cast<f32x4*>(&lds[L_RA + w * 512 + 8 * lane + 4]) = f32x4{a[4], a[5], a[6], a[7]};
  }
  __syncthreads();
  {
    float sa = 0.f;
#pragma unroll
    for (int i = 0; i < NW; ++i) sa += lds[L_RA + i * 512 + tid];
    pa[(size_t)(b * NH + h) * DM + tid] = sa;
    if (ffn) {
      float sb = 0.f;
#pragma unroll
      for (int i = 0; i < NW; ++i) sb += lds[L_RB + i * 512 + tid];
      pb[(size_t)(b * (NH / 2) + (h >> 1)) * DM + tid] = sb;
    }
  }
}

__global__ void __launch_bounds__(128) reduce_kernel(const uint16_t* __restrict__ x, const float* __restrict__ pa,
                                                     const float* __restrict__ pb, uint16_t* __restrict__ out) {
  const int idx = blockIdx.x * 128 + threadIdx.x;
  const int b = idx >> 9, n = idx & 511;
  float va[NH], vb[NH / 2];
#pragma unroll
  for (int i = 0; i < NH; ++i) va[i] = pa[(size_t)(b * NH + i) * DM + n];
#pragma unroll
  for (int i = 0; i < NH / 2; ++i) vb[i] = pb[(size_t)(b * (NH / 2) + i) * DM + n];
  float sa = 0.f, sb = 0.f;
#pragma unroll
  for (int i = 0; i < NH; ++i) sa += va[i];
#pragma unroll
  for (int i = 0; i < NH / 2; ++i) sb += vb[i];
  const float xv = __builtin_bit_cast(float, (unsigned)x[idx] << 16);
  const float o = bfr(bfr(xv + bfr(sa)) + bfr(sb));
  out[idx] = (uint16_t)(__builtin_bit_cast(unsigned, o) >> 16);
}

extern "C" void kernel_launch(void* const* d_in, const int* in_sizes, int n_in, void* d_out, int out_size,
                              void* d_ws, size_t ws_size, hipStream_t stream) {
  const uint16_t* x = (const uint16_t*)d_in[0];
  const uint16_t* kc = (const uint16_t*)d_in[1];
  const uint16_t* vc = (const uint16_t*)d_in[2];
  const uint16_t* wq = (const uint16_t*)d_in[3];
  const uint16_t* wo = (const uint16_t*)d_in[4];
  const uint16_t* wg = (const uint16_t*)d_in[6];
  const uint16_t* wu = (const uint16_t*)d_in[7];
  const uint16_t* wd = (const uint16_t*)d_in[8];
  float* pa = (float*)d_ws;
  float* pb = pa + NB * NH * DM;
  fused_decode_kernel<<<NB * NH, NT, 0, stream>>>(x, kc, vc, wq, wo, wg, wu, wd, pa, pb);
  reduce_kernel<<<NB * DM / 128, 128, 0, stream>>>(x, pa, pb, (uint16_t*)d_out);
}
```

```cpp
#define CFG_IB 96
#define CFG_D 8
#define CFG_PRE 16
#include <hip/hip_runtime.h>
#include <hip/hip_bf16.h>
#include <stdint.h>

typedef unsigned u32x4 __attribute__((ext_vector_type(4)));
typedef unsigned u32x2 __attribute__((ext_vector_type(2)));
typedef float f32x4 __attribute__((ext_vector_type(4)));
typedef float f32x2 __attribute__((ext_vector_type(2)));
typedef __bf16 bf16x2 __attribute__((ext_vector_type(2)));

constexpr int NB = 8, DM = 512, NH = 32, SK = 4096, DH = 128, FF = 1376, FS = 43;
constexpr int NW = 8, NT = NW * 64;
#define CFG_CK 4
constexpr int CK = CFG_CK;
constexpr int CL = CK / 4;
constexpr int IB = CFG_IB;
constexpr int IL = IB / 4;
constexpr int NCHT = (SK - 4 * IB) / CK;
constexpr int NQ = NCHT / 8 + CFG_D / 2;
constexpr int NK = NCHT / 8 - CFG_D / 2;
#define CFG_NS 64
constexpr int NS = CFG_NS;
#define CFG_STAG (32 / CFG_CK)
constexpr int STAG = CFG_STAG;
constexpr int RW = DM / NW;
constexpr int QW = 4;
constexpr int QRW = DM / QW;
constexpr int QR = QRW / 4;
constexpr int FR = RW / 8;
constexpr int OR = DH / NW;
constexpr int DR = (FS + NW - 1) / NW;
constexpr float SCALE = 0.08838834764831845f;
constexpr float LOG2E = 1.4426950408889634f;
static_assert(STAG % 2 == 0 && STAG * (NW - 1) + 6 <= NS && NS % 2 == 0 && NS + 2 <= NK && NQ % 2 == 0 && NK % 2 == 0 && 4 * (NQ + NK) == NCHT && RW == 64, "config");

constexpr int L_XN = 0;
constexpr int L_QP = L_XN + NW * 32;
constexpr int L_QB = L_QP + NW * 128;
constexpr int L_FP = L_QB + 64;
constexpr int L_REC = L_FP + NW * 128;
constexpr int L_RA = L_REC + NW * 132;
constexpr int L_RB = L_RA + NW * 512;
constexpr int L_CT = L_RB + NW * 512;
constexpr int L_END = L_CT + 4;

__device__ __forceinline__ float bfr(float x) { return __bfloat162float(__float2bfloat16(x)); }
__device__ __forceinline__ float bf_lo(unsigned u) { return __builtin_bit_cast(float, u << 16); }
__device__ __forceinline__ float bf_hi(unsigned u) { return __builtin_bit_cast(float, u & 0xffff0000u); }
__device__ __forceinline__ float dot2(unsigned k, unsigned q, float acc) {
  return __builtin_amdgcn_fdot2_f32_bf16(__builtin_bit_cast(bf16x2, k), __builtin_bit_cast(bf16x2, q), acc, false);
}
__device__ __forceinline__ void axpy8(float (&acc)[8], const u32x4& row, unsigned slo, unsigned shi) {
#pragma unroll
  for (int j = 0; j < 4; ++j) { acc[2 * j] = dot2(row[j], slo, acc[2 * j]); acc[2 * j + 1] = dot2(row[j], shi, acc[2 * j + 1]); }
}
template <int CTRL> __device__ __forceinline__ float dpp(float x) {
  return __builtin_bit_cast(float, __builtin_amdgcn_mov_dpp(__builtin_bit_cast(int, x), CTRL, 0xf, 0xf, true));
}
constexpr int ROR1 = 0x121, ROR2 = 0x122, ROR4 = 0x124, ROR8 = 0x128;
__device__ __forceinline__ float row16_sum(float x) {
  x += dpp<ROR8>(x); x += dpp<ROR4>(x); x += dpp<ROR2>(x); x += dpp<ROR1>(x); return x;
}
__device__ __forceinline__ float xrow_sum(float x) {
  auto s = __builtin_amdgcn_permlane16_swap(__float_as_uint(x), __float_as_uint(x), false, false);
  x = __uint_as_float(s[0]) + __uint_as_float(s[1]);
  auto t = __builtin_amdgcn_permlane32_swap(__float_as_uint(x), __float_as_uint(x), false, false);
  return __uint_as_float(t[0]) + __uint_as_float(t[1]);
}
__device__ __forceinline__ float xrow_max(float x) {
  auto s = __builtin_amdgcn_permlane16_swap(__float_as_uint(x), __float_as_uint(x), false, false);
  x = fmaxf(__uint_as_float(s[0]), __uint_as_float(s[1]));
  auto t = __builtin_amdgcn_permlane32_swap(__float_as_uint(x), __float_as_uint(x), false, false);
  return fmaxf(__uint_as_float(t[0]), __uint_as_float(t[1]));
}
__device__ __forceinline__ u32x4 ldnt(const void* p) { return __builtin_nontemporal_load(reinterpret_cast<const u32x4*>(p)); }
__device__ __forceinline__ u32x4 ld16(const void* p) { return *reinterpret_cast<const u32x4*>(p); }
__device__ __forceinline__ u32x4 ldw(const void* p) { return ld16(p); }
#define SCHED_FENCE() __builtin_amdgcn_sched_barrier(0)
#define MEM_FENCE() asm volatile("" ::: "memory")

__global__ void __launch_bounds__(NT) fused_decode_kernel(
    const uint16_t* __restrict__ x, const uint16_t* __restrict__ kc, const uint16_t* __restrict__ vc,
    const uint16_t* __restrict__ wq, const uint16_t* __restrict__ wo, const uint16_t* __restrict__ wg,
    const uint16_t* __restrict__ wu, const uint16_t* __restrict__ wd, float* __restrict__ pa,
    float* __restrict__ pb) {
  __shared__ __attribute__((aligned(16))) float lds[L_END];
  const int h = blockIdx.x & (NH - 1), b = blockIdx.x >> 5;
  const int tid = threadIdx.x, lane = tid & 63, w = __builtin_amdgcn_readfirstlane(tid >> 6);
  const int r = lane >> 4, c = lane & 15, r8 = lane >> 3, c8 = lane & 7;

  const char* kbase = reinterpret_cast<const char*>(kc) + (size_t)(b * NH + h) * (SK * DH * 2) + (size_t)(4 * IB) * (DH * 2);
  const char* vbase = reinterpret_cast<const char*>(vc) + (size_t)(b * NH + h) * (SK * DH * 2) + (size_t)(4 * IB) * (DH * 2);
  u32x4 KI[IL], VI[IL];
  const unsigned loff = lane * 16;
  constexpr size_t CSTRIDE = (size_t)CK * DH * 2;
  unsigned* ctr = reinterpret_cast<unsigned*>(&lds[L_CT]);
  u32x4 KA[CL], VA[CL], KB[CL], VB[CL];
#define ISSUE(KR, VR, ch) do { _Pragma("unroll") for (int i_ = 0; i_ < CL; ++i_) KR[i_] = ldnt(kbase + (size_t)(ch) * CSTRIDE + i_ * 1024 + loff); \
                               _Pragma("unroll") for (int i_ = 0; i_ < CL; ++i_) VR[i_] = ldnt(vbase + (size_t)(ch) * CSTRIDE + i_ * 1024 + loff); } while (0)
  uint16_t* xn16 = reinterpret_cast<uint16_t*>(&lds[L_XN]);
  if (tid == NT - 1) ctr[0] = 0u;
  if (w < QW) {
    const uint16_t* xb = x + b * DM;
    const u32x4 xall = ld16(xb + 8 * lane);
    const unsigned xm0 = xb[QRW * w + lane], xm1 = xb[QRW * w + 64 + lane];
    u32x4 wqv[QR];
    {
      const char* base = reinterpret_cast<const char*>(wq) + (size_t)(QRW * w) * (NH * DH * 2) + h * (DH * 2);
      const unsigned voff = (unsigned)(QR * r) * (NH * DH * 2) + 16 * c;
#pragma unroll
      for (int i = 0; i < QR; ++i) wqv[i] = ldw(base + (size_t)i * (NH * DH * 2) + voff);
    }
    SCHED_FENCE();
    __builtin_amdgcn_s_barrier();
    SCHED_FENCE();
    {
      float xs[8];
#pragma unroll
      for (int j = 0; j < 4; ++j) { xs[2 * j] = bf_lo(xall[j]); xs[2 * j + 1] = bf_hi(xall[j]); }
      float s = ((xs[0] + xs[1]) + (xs[2] + xs[3])) + ((xs[4] + xs[5]) + (xs[6] + xs[7]));
      s = xrow_sum(row16_sum(s));
      const float mean = s * (1.0f / DM);
      float v = 0.f;
#pragma unroll
      for (int j = 0; j < 8; ++j) { const float d = xs[j] - mean; v = fmaf(d, d, v); }
      v = xrow_sum(row16_sum(v));
      const float var_b = bfr(v * (1.0f / DM));
      const float mean_b = bfr(mean);
      const float den = bfr(sqrtf(bfr(var_b + 1e-5f)));
      const float xn0 = bfr(bfr(bf_lo(xm0) - mean_b) / den);
      const float xn1 = bfr(bfr(bf_lo(xm1) - mean_b) / den);
      xn16[QRW * w + lane] = (uint16_t)(__builtin_bit_cast(unsigned, xn0) >> 16);
      xn16[QRW * w + 64 + lane] = (uint16_t)(__builtin_bit_cast(unsigned, xn1) >> 16);
    }
    u32x4 xa[QR / 8];
#pragma unroll
    for (int i = 0; i < QR / 8; ++i) xa[i] = *reinterpret_cast<const u32x4*>(xn16 + QRW * w + QR * r + 8 * i);
    float acc[8];
#pragma unroll
    for (int j = 0; j < 8; ++j) acc[j] = 0.f;
#pragma unroll
    for (int i = 0; i < QR; ++i) {
      const unsigned p = xa[i >> 3][(i >> 1) & 3];
      axpy8(acc, wqv[i], (i & 1) ? (p >> 16) : (p & 0xffffu), (i & 1) ? (p & 0xffff0000u) : (p << 16));
    }
#pragma unroll
    for (int j = 0; j < 8; ++j) acc[j] = xrow_sum(acc[j]);
    if (r == 0) {
      *reinterpret_cast<f32x4*>(&lds[L_QP + w * 128 + 8 * c]) = f32x4{acc[0], acc[1], acc[2], acc[3]};
      *reinterpret_cast<f32x4*>(&lds[L_QP + w * 128 + 8 * c + 4]) = f32x4{acc[4], acc[5], acc[6], acc[7]};
    }
  } else {
    const char* ki = reinterpret_cast<const char*>(kc) + (size_t)(b * NH + h) * (SK * DH * 2) + (size_t)((w - QW) * IB) * (DH * 2);
    const char* vi = reinterpret_cast<const char*>(vc) + (size_t)(b * NH + h) * (SK * DH * 2) + (size_t)((w - QW) * IB) * (DH * 2);
#pragma unroll
    for (int i = 0; i < 2 * IL; ++i) {
      if (i == CFG_PRE) { SCHED_FENCE(); __builtin_amdgcn_s_barrier(); SCHED_FENCE(); }
      if (i < IL) KI[i] = ldnt(ki + i * 1024 + loff); else VI[i - IL] = ldnt(vi + (i - IL) * 1024 + loff);
    }
    if (CFG_PRE >= 2 * IL) { SCHED_FENCE(); __builtin_amdgcn_s_barrier(); }
  }
  SCHED_FENCE();
  __syncthreads();
  if (tid < 128) {
    float s = 0.f;
#pragma unroll
    for (int i = 0; i < QW; ++i) s += lds[L_QP + i * 128 + tid];
    reinterpret_cast<uint16_t*>(&lds[L_QB])[tid] = (uint16_t)(__builtin_bit_cast(unsigned, bfr(s)) >> 16);
  }
  SCHED_FENCE();
  __syncthreads();
  ISSUE(KA, VA, w);
  ISSUE(KB, VB, NW + w);
  SCHED_FENCE();
  const u32x4 q = *reinterpret_cast<const u32x4*>(&lds[L_QB + 4 * c]);

  float m = -INFINITY, l = 0.f, acc[8];
#pragma unroll
  for (int j = 0; j < 8; ++j) acc[j] = 0.f;
#define CONSUME(KR, VR) CONSUMEN(KR, VR, CL)
#define CONSUMEN(KR, VR, CL) do { \
    float s_[CL]; float mn_ = m; \
    _Pragma("unroll") for (int i_ = 0; i_ < CL; ++i_) { \
      float d_ = dot2(KR[i_][0], q[0], 0.f); d_ = dot2(KR[i_][1], q[1], d_); d_ = dot2(KR[i_][2], q[2], d_); d_ = dot2(KR[i_][3], q[3], d_); \
      d_ = row16_sum(d_); s_[i_] = bfr(bfr(d_) * SCALE) * LOG2E; mn_ = fmaxf(mn_, s_[i_]); } \
    const float al_ = __builtin_amdgcn_exp2f(m - mn_); m = mn_; \
    float ps_ = 0.f; \
    _Pragma("unroll") for (int i_ = 0; i_ < CL; ++i_) { s_[i_] = __builtin_amdgcn_exp2f(s_[i_] - mn_); ps_ += s_[i_]; } \
    l = fmaf(l, al_, ps_); \
    _Pragma("unroll") for (int j_ = 0; j_ < 8; ++j_) acc[j_] *= al_; \
    _Pragma("unroll") for (int i_ = 0; i_ < CL; ++i_) { \
      _Pragma("unroll") for (int j_ = 0; j_ < 4; ++j_) { \
        acc[2 * j_] = fmaf(s_[i_], bf_lo(VR[i_][j_]), acc[2 * j_]); \
        acc[2 * j_ + 1] = fmaf(s_[i_], bf_hi(VR[i_][j_]), acc[2 * j_ + 1]); } } \
    asm volatile("" : "+v"(acc[0]), "+v"(acc[1]), "+v"(acc[2]), "+v"(acc[3]), "+v"(acc[4]), "+v"(acc[5]), "+v"(acc[6]), "+v"(acc[7]), "+v"(l), "+v"(m)); \
  } while (0)
#define CIDX(i) (((i) < NK ? NW * (i) : (NW - QW) * NK + QW * (i)) + w)
#define STEP2(it) do { \
    CONSUME(KA, VA); SCHED_FENCE(); ISSUE(KA, VA, CIDX((it) + 2)); SCHED_FENCE(); \
    CONSUME(KB, VB); SCHED_FENCE(); ISSUE(KB, VB, CIDX((it) + 3)); SCHED_FENCE(); } while (0)

  if (w >= QW) {
#pragma unroll
    for (int i = 0; i < IL; i += 4) { CONSUMEN((KI + i), (VI + i), 4); SCHED_FENCE(); }
  }
  const int it_w = STAG * w;
#pragma unroll 1
  for (int it = 0; it < it_w; it += 2) STEP2(it);
  const int c0 = (FS * h) >> 3;
  const int foff = FS * h - 8 * c0;
  u32x4 gv[FR], uv[FR];
  {
    const unsigned voff = (unsigned)(FR * r8) * (FF * 2) + 16 * min(c0 + c8, FF / 8 - 1);
    const char* gb = reinterpret_cast<const char*>(wg) + (size_t)(RW * w) * (FF * 2);
    const char* ub = reinterpret_cast<const char*>(wu) + (size_t)(RW * w) * (FF * 2);
#pragma unroll
    for (int i = 0; i < FR; ++i) { gv[i] = ldw(gb + (size_t)i * (FF * 2) + voff); uv[i] = ldw(ub + (size_t)i * (FF * 2) + voff); }
  }
  SCHED_FENCE();
  STEP2(it_w);
  {
    const u32x4 xa = *reinterpret_cast<const u32x4*>(xn16 + RW * w + FR * r8);
    float ag[8], au[8];
#pragma unroll
    for (int j = 0; j < 8; ++j) { ag[j] = 0.f; au[j] = 0.f; }
#pragma unroll
    for (int i = 0; i < FR; ++i) {
      const unsigned p = xa[i >> 1];
      const unsigned slo = (i & 1) ? (p >> 16) : (p & 0xffffu), shi = (i & 1) ? (p & 0xffff0000u) : (p << 16);
      axpy8(ag, gv[i], slo, shi);
      axpy8(au, uv[i], slo, shi);
    }
#pragma unroll
    for (int j = 0; j < 8; ++j) {
      ag[j] += dpp<ROR8>(ag[j]); ag[j] = xrow_sum(ag[j]);
      au[j] += dpp<ROR8>(au[j]); au[j] = xrow_sum(au[j]);
    }
    if (r8 == 0) {
      float* fp = &lds[L_FP + w * 128 + 8 * c8];
      *reinterpret_cast<f32x4*>(fp) = f32x4{ag[0], ag[1], ag[2], ag[3]};
      *reinterpret_cast<f32x4*>(fp + 4) = f32x4{ag[4], ag[5], ag[6], ag[7]};
      *reinterpret_cast<f32x4*>(fp + 64) = f32x4{au[0], au[1], au[2], au[3]};
      *reinterpret_cast<f32x4*>(fp + 68) = f32x4{au[4], au[5], au[6], au[7]};
    }
    MEM_FENCE();
    if (lane == 0) __hip_atomic_fetch_add(&ctr[0], 1u, __ATOMIC_RELAXED, __HIP_MEMORY_SCOPE_WORKGROUP);
    MEM_FENCE();
  }
  SCHED_FENCE();
  u32x4 wov[OR], wdv[DR];
  {
    const char* ob = reinterpret_cast<const char*>(wo) + (size_t)(h * DH + OR * w) * (DM * 2);
    const char* db = reinterpret_cast<const char*>(wd) + (size_t)(FS * h) * (DM * 2);
#pragma unroll
    for (int i = 0; i < OR; ++i) wov[i] = ldw(ob + i * (DM * 2) + loff);
#pragma unroll
    for (int i = 0; i < DR; ++i) wdv[i] = ldw(db + (size_t)min(w + NW * i, FS - 1) * (DM * 2) + loff);
  }
  SCHED_FENCE();
  {
    int it = it_w + 2;
#pragma unroll 1
    do { STEP2(it); it += 2; } while (it < NS - 2);
  }
  {
    while (__hip_atomic_load(&ctr[0], __ATOMIC_RELAXED, __HIP_MEMORY_SCOPE_WORKGROUP) < (unsigned)NW) __builtin_amdgcn_s_sleep(1);
    MEM_FENCE();
    float g = 0.f, u = 0.f;
    const int jr = min(w + NW * (lane & 7), FS - 1);
#pragma unroll
    for (int i = 0; i < NW; ++i) {
      g += lds[L_FP + i * 128 + foff + jr];
      u += lds[L_FP + i * 128 + 64 + foff + jr];
    }
    g = bfr(g); u = bfr(u);
    g = g > 0.f ? g : 0.f;
    const unsigned hb = __builtin_bit_cast(unsigned, bfr(g * u));
    float bb[8];
#pragma unroll
    for (int j = 0; j < 8; ++j) bb[j] = 0.f;
#pragma unroll
    for (int t = 0; t < DR; ++t) {
      unsigned s = __builtin_amdgcn_readlane(hb, t);
      if (w + NW * t >= FS) s = 0u;
      axpy8(bb, wdv[t], s >> 16, s);
    }
    *reinterpret_cast<f32x4*>(&lds[L_RB + w * 512 + 8 * lane]) = f32x4{bb[0], bb[1], bb[2], bb[3]};
    *reinterpret_cast<f32x4*>(&lds[L_RB + w * 512 + 8 * lane + 4]) = f32x4{bb[4], bb[5], bb[6], bb[7]};
#pragma unroll
    for (int i = 0; i < OR; i += 2) {
#pragma unroll
      for (int j = 0; j < 4; ++j) {
        const unsigned r0 = wov[i][j], r1 = wov[i + 1][j];
        wov[i][j] = __builtin_amdgcn_perm(r1, r0, 0x05040100u);
        wov[i + 1][j] = __builtin_amdgcn_perm(r1, r0, 0x07060302u);
      }
      asm volatile("" : "+v"(wov[i]), "+v"(wov[i + 1]));
    }
  }
  SCHED_FENCE();
  {
    const int n_w = w < QW ? NQ : NK;
    int it = NS - 2;
#pragma unroll 1
    do { STEP2(it); it += 2; } while (it < n_w - 2);
  }
  CONSUME(KA, VA);
  SCHED_FENCE();
  CONSUME(KB, VB);
#undef STEP2
#undef CIDX
#undef CONSUME
#undef CONSUMEN
#undef ISSUE

  {
    const float M = xrow_max(m);
    const float wt = __builtin_amdgcn_exp2f(m - M);
    l = xrow_sum(l * wt);
#pragma unroll
    for (int j = 0; j < 8; ++j) acc[j] = xrow_sum(acc[j] * wt);
    if (r == 0) {
      float* rec = &lds[L_REC + w * 132];
      *reinterpret_cast<f32x4*>(rec + 4 + 8 * c) = f32x4{acc[0], acc[1], acc[2], acc[3]};
      *reinterpret_cast<f32x4*>(rec + 8 + 8 * c) = f32x4{acc[4], acc[5], acc[6], acc[7]};
      if (c == 0) *reinterpret_cast<f32x2*>(rec) = f32x2{M, l};
    }
  }
  SCHED_FENCE();
  __syncthreads();
  SCHED_FENCE();
  {
    f32x2 ml[NW];
    float av[NW];
#pragma unroll
    for (int i = 0; i < NW; ++i) {
      ml[i] = *reinterpret_cast<const f32x2*>(&lds[L_REC + i * 132]);
      av[i] = lds[L_REC + i * 132 + 4 + OR * w + c];
    }
    float M = ml[0][0];
#pragma unroll
    for (int i = 1; i < NW; ++i) M = fmaxf(M, ml[i][0]);
    float L = 0.f, o = 0.f;
#pragma unroll
    for (int i = 0; i < NW; ++i) {
      const float wt = __builtin_amdgcn_exp2f(ml[i][0] - M);
      L = fmaf(wt, ml[i][1], L);
      o = fmaf(wt, av[i], o);
    }
    const unsigned ob = __builtin_bit_cast(unsigned, bfr(o / L));
    const unsigned nx = __builtin_amdgcn_mov_dpp(ob, 0x101  , 0xf, 0xf, true);
    const unsigned op = __builtin_amdgcn_perm(nx, ob, 0x07060302u);
    float a[8];
#pragma unroll
    for (int j = 0; j < 8; ++j) a[j] = 0.f;
#pragma unroll
    for (int i = 0; i < OR; i += 2) {
      const unsigned s = __builtin_amdgcn_readlane(op, i);
#pragma unroll
      for (int j = 0; j < 4; ++j) { a[2 * j] = dot2(wov[i][j], s, a[2 * j]); a[2 * j + 1] = dot2(wov[i + 1][j], s, a[2 * j + 1]); }
    }
    *reinterpret_cast<f32x4*>(&lds[L_RA + w * 512 + 8 * lane]) = f32x4{a[0], a[1], a[2], a[3]};
    *reinterpret_cast<f32x4*>(&lds[L_RA + w * 512 + 8 * lane + 4]) = f32x4{a[4], a[5], a[6], a[7]};
  }
  __syncthreads();
  {
    float sa = 0.f, sb = 0.f;
#pragma unroll
    for (int i = 0; i < NW; ++i) { sa += lds[L_RA + i * 512 + tid]; sb += lds[L_RB + i * 512 + tid]; }
    pa[(size_t)(b * NH + h) * DM + tid] = sa;
    pb[(size_t)(b * NH + h) * DM + tid] = sb;
  }
}

__global__ void __launch_bounds__(128) reduce_kernel(const uint16_t* __restrict__ x, const float* __restrict__ pa,
                                                     const float* __restrict__ pb, uint16_t* __restrict__ out) {
  const int idx = blockIdx.x * 128 + threadIdx.x;
  const int b = idx >> 9, n = idx & 511;
  float va[NH], vb[NH];
#pragma unroll
  for (int i = 0; i < NH; ++i) { va[i] = pa[(size_t)(b * NH + i) * DM + n]; vb[i] = pb[(size_t)(b * NH + i) * DM + n]; }
  float sa = 0.f, sb = 0.f;
#pragma unroll
  for (int i = 0; i < NH; ++i) { sa += va[i]; sb += vb[i]; }
  const float xv = __builtin_bit_cast(float, (unsigned)x[idx] << 16);
  const float o = bfr(bfr(xv + bfr(sa)) + bfr(sb));
  out[idx] = (uint16_t)(__builtin_bit_cast(unsigned, o) >> 16);
}

extern "C" void kernel_launch(void* const* d_in, const int* in_sizes, int n_in, void* d_out, int out_size,
                              void* d_ws, size_t ws_size, hipStream_t stream) {
  const uint16_t* x = (const uint16_t*)d_in[0];
  const uint16_t* kc = (const uint16_t*)d_in[1];
  const uint16_t* vc = (const uint16_t*)d_in[2];
  const uint16_t* wq = (const uint16_t*)d_in[3];
  const uint16_t* wo = (const uint16_t*)d_in[4];
  const uint16_t* wg = (const uint16_t*)d_in[6];
  const uint16_t* wu = (const uint16_t*)d_in[7];
  const uint16_t* wd = (const uint16_t*)d_in[8];
  float* pa = (float*)d_ws;
  float* pb = pa + NB * NH * DM;
  fused_decode_kernel<<<NB * NH, NT, 0, stream>>>(x, kc, vc, wq, wo, wg, wu, wd, pa, pb);
  reduce_kernel<<<NB * DM / 128, 128, 0, stream>>>(x, pa, pb, (uint16_t*)d_out);
}
```

```cpp
#define CFG_D 16
#define CFG_IL 40
#include <hip/hip_runtime.h>
#include <hip/hip_bf16.h>
#include <stdint.h>

typedef unsigned u32x4 __attribute__((ext_vector_type(4)));
typedef unsigned u32x2 __attribute__((ext_vector_type(2)));
typedef float f32x4 __attribute__((ext_vector_type(4)));
typedef float f32x2 __attribute__((ext_vector_type(2)));
typedef __bf16 bf16x2 __attribute__((ext_vector_type(2)));

constexpr int NB = 8, DM = 512, NH = 32, SK = 4096, DH = 128, FF = 1376, FS = 43;
constexpr int NW = 8, NT = NW * 64;
constexpr int NC = SK / 4;
constexpr int IL = CFG_IL;
#define CFG_PRE 0
constexpr int NCHT = NC - 4 * IL;
constexpr int NQ = NCHT / 8 + CFG_D / 2;
constexpr int NK = NCHT / 8 - CFG_D / 2;
constexpr int NV = NC / NW;
#define CFG_STAG 8
constexpr int STAG = CFG_STAG;
#define CFG_STAG2 4
constexpr int STAG2 = CFG_STAG2;
constexpr int RW = DM / NW;
constexpr int QW = 4;
constexpr int QRW = DM / QW;
constexpr int QR = QRW / 4;
constexpr int FR = RW / 8;
constexpr int OR = DH / NW;
constexpr int DR = (FS + NW - 1) / NW;
constexpr float SCALE = 0.08838834764831845f;
constexpr float LOG2E = 1.4426950408889634f;
static_assert(STAG % 4 == 0 && STAG * (NW - 1) + 16 <= NK && NQ % 4 == 0 && NK % 4 == 0 && 4 * (NQ + NK) == NCHT && RW == 64 &&
              STAG2 % 4 == 0 && STAG2 > 0 && STAG2 * NW + 8 <= NV && IL % 4 == 0, "config");

constexpr int L_XN = 0;
constexpr int L_QP = L_XN + NW * 32;
constexpr int L_QB = L_QP + NW * 128;
constexpr int L_FP = L_QB + 64;
constexpr int L_REC = L_FP + NW * 128;
constexpr int L_RA = L_REC + NW * 132;
constexpr int L_RB = L_RA + NW * 512;
constexpr int L_MX = L_RB + NW * 512;
constexpr int L_S = L_MX + NW;
constexpr int L_END = L_S + SK;

__device__ __forceinline__ float bfr(float x) { return __bfloat162float(__float2bfloat16(x)); }
__device__ __forceinline__ float bf_lo(unsigned u) { return __builtin_bit_cast(float, u << 16); }
__device__ __forceinline__ float bf_hi(unsigned u) { return __builtin_bit_cast(float, u & 0xffff0000u); }
__device__ __forceinline__ float dot2(unsigned k, unsigned q, float acc) {
  return __builtin_amdgcn_fdot2_f32_bf16(__builtin_bit_cast(bf16x2, k), __builtin_bit_cast(bf16x2, q), acc, false);
}
__device__ __forceinline__ void axpy8(float (&acc)[8], const u32x4& row, unsigned slo, unsigned shi) {
#pragma unroll
  for (int j = 0; j < 4; ++j) { acc[2 * j] = dot2(row[j], slo, acc[2 * j]); acc[2 * j + 1] = dot2(row[j], shi, acc[2 * j + 1]); }
}
template <int CTRL> __device__ __forceinline__ float dpp(float x) {
  return __builtin_bit_cast(float, __builtin_amdgcn_mov_dpp(__builtin_bit_cast(int, x), CTRL, 0xf, 0xf, true));
}
constexpr int ROR1 = 0x121, ROR2 = 0x122, ROR4 = 0x124, ROR8 = 0x128;
__device__ __forceinline__ float row16_sum(float x) {
  x += dpp<ROR8>(x); x += dpp<ROR4>(x); x += dpp<ROR2>(x); x += dpp<ROR1>(x); return x;
}
__device__ __forceinline__ float xrow_sum(float x) {
  auto s = __builtin_amdgcn_permlane16_swap(__float_as_uint(x), __float_as_uint(x), false, false);
  x = __uint_as_float(s[0]) + __uint_as_float(s[1]);
  auto t = __builtin_amdgcn_permlane32_swap(__float_as_uint(x), __float_as_uint(x), false, false);
  return __uint_as_float(t[0]) + __uint_as_float(t[1]);
}
__device__ __forceinline__ float xrow_max(float x) {
  auto s = __builtin_amdgcn_permlane16_swap(__float_as_uint(x), __float_as_uint(x), false, false);
  x = fmaxf(__uint_as_float(s[0]), __uint_as_float(s[1]));
  auto t = __builtin_amdgcn_permlane32_swap(__float_as_uint(x), __float_as_uint(x), false, false);
  return fmaxf(__uint_as_float(t[0]), __uint_as_float(t[1]));
}
__device__ __forceinline__ u32x4 ldnt(const void* p) { return __builtin_nontemporal_load(reinterpret_cast<const u32x4*>(p)); }
__device__ __forceinline__ u32x4 ld16(const void* p) { return *reinterpret_cast<const u32x4*>(p); }
__device__ __forceinline__ u32x4 ldw(const void* p) { return ld16(p); }
#define SCHED_FENCE() __builtin_amdgcn_sched_barrier(0)
#define MEM_FENCE() asm volatile("" ::: "memory")

__global__ void __launch_bounds__(NT) fused_decode_kernel(
    const uint16_t* __restrict__ x, const uint16_t* __restrict__ kc, const uint16_t* __restrict__ vc,
    const uint16_t* __restrict__ wq, const uint16_t* __restrict__ wo, const uint16_t* __restrict__ wg,
    const uint16_t* __restrict__ wu, const uint16_t* __restrict__ wd, float* __restrict__ pa,
    float* __restrict__ pb) {
  __shared__ __attribute__((aligned(16))) float lds[L_END];
  const int h = blockIdx.x & (NH - 1), b = blockIdx.x >> 5;
  const int tid = threadIdx.x, lane = tid & 63, w = __builtin_amdgcn_readfirstlane(tid >> 6);
  const int r = lane >> 4, c = lane & 15, r8 = lane >> 3, c8 = lane & 7;

  const char* kbase = reinterpret_cast<const char*>(kc) + (size_t)(b * NH + h) * (SK * DH * 2);
  const char* vbase = reinterpret_cast<const char*>(vc) + (size_t)(b * NH + h) * (SK * DH * 2);
  u32x4 KI[IL];
  const unsigned loff = lane * 16;
  u32x4 B0, B1, B2, B3;
  uint16_t* xn16 = reinterpret_cast<uint16_t*>(&lds[L_XN]);
  if (w < QW) {
    const uint16_t* xb = x + b * DM;
    const u32x4 xall = ld16(xb + 8 * lane);
    const unsigned xm0 = xb[QRW * w + lane], xm1 = xb[QRW * w + 64 + lane];
    u32x4 wqv[QR];
    {
      const char* base = reinterpret_cast<const char*>(wq) + (size_t)(QRW * w) * (NH * DH * 2) + h * (DH * 2);
      const unsigned voff = (unsigned)(QR * r) * (NH * DH * 2) + 16 * c;
#pragma unroll
      for (int i = 0; i < QR; ++i) wqv[i] = ldw(base + (size_t)i * (NH * DH * 2) + voff);
    }
    SCHED_FENCE();
    __builtin_amdgcn_s_barrier();
    SCHED_FENCE();
    {
      float xs[8];
#pragma unroll
      for (int j = 0; j < 4; ++j) { xs[2 * j] = bf_lo(xall[j]); xs[2 * j + 1] = bf_hi(xall[j]); }
      float s = ((xs[0] + xs[1]) + (xs[2] + xs[3])) + ((xs[4] + xs[5]) + (xs[6] + xs[7]));
      s = xrow_sum(row16_sum(s));
      const float mean = s * (1.0f / DM);
      float v = 0.f;
#pragma unroll
      for (int j = 0; j < 8; ++j) { const float d = xs[j] - mean; v = fmaf(d, d, v); }
      v = xrow_sum(row16_sum(v));
      const float var_b = bfr(v * (1.0f / DM));
      const float mean_b = bfr(mean);
      const float den = bfr(sqrtf(bfr(var_b + 1e-5f)));
      const float xn0 = bfr(bfr(bf_lo(xm0) - mean_b) / den);
      const float xn1 = bfr(bfr(bf_lo(xm1) - mean_b) / den);
      xn16[QRW * w + lane] = (uint16_t)(__builtin_bit_cast(unsigned, xn0) >> 16);
      xn16[QRW * w + 64 + lane] = (uint16_t)(__builtin_bit_cast(unsigned, xn1) >> 16);
    }
    u32x4 xa[QR / 8];
#pragma unroll
    for (int i = 0; i < QR / 8; ++i) xa[i] = *reinterpret_cast<const u32x4*>(xn16 + QRW * w + QR * r + 8 * i);
    float acc[8];
#pragma unroll
    for (int j = 0; j < 8; ++j) acc[j] = 0.f;
#pragma unroll
    for (int i = 0; i < QR; ++i) {
      const unsigned p = xa[i >> 3][(i >> 1) & 3];
      axpy8(acc, wqv[i], (i & 1) ? (p >> 16) : (p & 0xffffu), (i & 1) ? (p & 0xffff0000u) : (p << 16));
    }
#pragma unroll
    for (int j = 0; j < 8; ++j) acc[j] = xrow_sum(acc[j]);
    if (r == 0) {
      *reinterpret_cast<f32x4*>(&lds[L_QP + w * 128 + 8 * c]) = f32x4{acc[0], acc[1], acc[2], acc[3]};
      *reinterpret_cast<f32x4*>(&lds[L_QP + w * 128 + 8 * c + 4]) = f32x4{acc[4], acc[5], acc[6], acc[7]};
    }
  } else {
    const char* ki = kbase + (size_t)((w - QW) * IL) * 1024;
#pragma unroll
    for (int i = 0; i < IL; ++i) {
      if (i == CFG_PRE) { SCHED_FENCE(); __builtin_amdgcn_s_barrier(); SCHED_FENCE(); }
      KI[i] = ldnt(ki + i * 1024 + loff);
    }
  }
  SCHED_FENCE();
  __syncthreads();
  if (tid < 128) {
    float s = 0.f;
#pragma unroll
    for (int i = 0; i < QW; ++i) s += lds[L_QP + i * 128 + tid];
    reinterpret_cast<uint16_t*>(&lds[L_QB])[tid] = (uint16_t)(__builtin_bit_cast(unsigned, bfr(s)) >> 16);
  }
  SCHED_FENCE();
  __syncthreads();
#define KIDX(i) (4 * IL + ((i) < NK ? NW * (i) : (NW - QW) * NK + QW * (i)) + w)
#define VIDX(i) (NW * (i) + w)
#define LDK(i) ldnt(kbase + (size_t)KIDX(i) * 1024 + loff)
#define LDV(i) ldnt(vbase + (size_t)VIDX(i) * 1024 + loff)
  B0 = LDK(0); B1 = LDK(1); B2 = LDK(2); B3 = LDK(3);
  SCHED_FENCE();
  const u32x4 q = *reinterpret_cast<const u32x4*>(&lds[L_QB + 4 * c]);

  float mx = -INFINITY;
  float* srow = &lds[L_S + r];
#define CONS_K(KR, gc) do { \
    float d_ = dot2(KR[0], q[0], 0.f); d_ = dot2(KR[1], q[1], d_); d_ = dot2(KR[2], q[2], d_); d_ = dot2(KR[3], q[3], d_); \
    d_ = row16_sum(d_); d_ = bfr(bfr(d_) * SCALE) * LOG2E; mx = fmaxf(mx, d_); srow[4 * (gc)] = d_; \
    asm volatile("" : "+v"(mx)); } while (0)
#define STEP_K(it) do { \
    CONS_K(B0, KIDX(it));     SCHED_FENCE(); B0 = LDK((it) + 4); SCHED_FENCE(); \
    CONS_K(B1, KIDX((it) + 1)); SCHED_FENCE(); B1 = LDK((it) + 5); SCHED_FENCE(); \
    CONS_K(B2, KIDX((it) + 2)); SCHED_FENCE(); B2 = LDK((it) + 6); SCHED_FENCE(); \
    CONS_K(B3, KIDX((it) + 3)); SCHED_FENCE(); B3 = LDK((it) + 7); SCHED_FENCE(); } while (0)

  if (w >= QW) {
#pragma unroll
    for (int i = 0; i < IL; ++i) { CONS_K(KI[i], (w - QW) * IL + i); if ((i & 3) == 3) SCHED_FENCE(); }
  }
  const int it_w = STAG * w;
#pragma unroll 1
  for (int it = 0; it < it_w; it += 4) STEP_K(it);
  const int c0 = (FS * h) >> 3;
  const int foff = FS * h - 8 * c0;
  u32x4 gv[FR], uv[FR];
  {
    const unsigned voff = (unsigned)(FR * r8) * (FF * 2) + 16 * min(c0 + c8, FF / 8 - 1);
    const char* gb = reinterpret_cast<const char*>(wg) + (size_t)(RW * w) * (FF * 2);
    const char* ub = reinterpret_cast<const char*>(wu) + (size_t)(RW * w) * (FF * 2);
#pragma unroll
    for (int i = 0; i < FR; ++i) { gv[i] = ldw(gb + (size_t)i * (FF * 2) + voff); uv[i] = ldw(ub + (size_t)i * (FF * 2) + voff); }
  }
  SCHED_FENCE();
  STEP_K(it_w);
  {
    const u32x4 xa = *reinterpret_cast<const u32x4*>(xn16 + RW * w + FR * r8);
    float ag[8], au[8];
#pragma unroll
    for (int j = 0; j < 8; ++j) { ag[j] = 0.f; au[j] = 0.f; }
#pragma unroll
    for (int i = 0; i < FR; ++i) {
      const unsigned p = xa[i >> 1];
      const unsigned slo = (i & 1) ? (p >> 16) : (p & 0xffffu), shi = (i & 1) ? (p & 0xffff0000u) : (p << 16);
      axpy8(ag, gv[i], slo, shi);
      axpy8(au, uv[i], slo, shi);
    }
#pragma unroll
    for (int j = 0; j < 8; ++j) {
      ag[j] += dpp<ROR8>(ag[j]); ag[j] = xrow_sum(ag[j]);
      au[j] += dpp<ROR8>(au[j]); au[j] = xrow_sum(au[j]);
    }
    if (r8 == 0) {
      float* fp = &lds[L_FP + w * 128 + 8 * c8];
      *reinterpret_cast<f32x4*>(fp) = f32x4{ag[0], ag[1], ag[2], ag[3]};
      *reinterpret_cast<f32x4*>(fp + 4) = f32x4{ag[4], ag[5], ag[6], ag[7]};
      *reinterpret_cast<f32x4*>(fp + 64) = f32x4{au[0], au[1], au[2], au[3]};
      *reinterpret_cast<f32x4*>(fp + 68) = f32x4{au[4], au[5], au[6], au[7]};
    }
  }
  SCHED_FENCE();
  u32x4 wov[OR], wdv[DR];
  {
    const char* ob = reinterpret_cast<const char*>(wo) + (size_t)(h * DH + OR * w) * (DM * 2);
    const char* db = reinterpret_cast<const char*>(wd) + (size_t)(FS * h) * (DM * 2);
#pragma unroll
    for (int i = 0; i < OR; ++i) wov[i] = ldw(ob + i * (DM * 2) + loff);
#pragma unroll
    for (int i = 0; i < DR; ++i) wdv[i] = ldw(db + (size_t)min(w + NW * i, FS - 1) * (DM * 2) + loff);
  }
  SCHED_FENCE();
  {
    const int n_w = w < QW ? NQ : NK;
    STEP_K(it_w + 4);
    int it = it_w + 8;
#pragma unroll 1
    do { STEP_K(it); it += 4; } while (it < n_w - 4);
    CONS_K(B0, KIDX(it));     SCHED_FENCE(); B0 = LDV(0); SCHED_FENCE();
    CONS_K(B1, KIDX(it + 1)); SCHED_FENCE(); B1 = LDV(1); SCHED_FENCE();
    CONS_K(B2, KIDX(it + 2)); SCHED_FENCE(); B2 = LDV(2); SCHED_FENCE();
    CONS_K(B3, KIDX(it + 3)); SCHED_FENCE(); B3 = LDV(3); SCHED_FENCE();
  }
  mx = xrow_max(mx);
  if (lane == 0) lds[L_MX + w] = mx;
  MEM_FENCE();
  asm volatile("s_waitcnt lgkmcnt(0)" ::: "memory");
  __builtin_amdgcn_s_barrier();
  MEM_FENCE();
  SCHED_FENCE();
  float M;
  {
    const f32x4 m0 = *reinterpret_cast<const f32x4*>(&lds[L_MX]), m1 = *reinterpret_cast<const f32x4*>(&lds[L_MX + 4]);
    M = fmaxf(fmaxf(fmaxf(m0[0], m0[1]), fmaxf(m0[2], m0[3])), fmaxf(fmaxf(m1[0], m1[1]), fmaxf(m1[2], m1[3])));
  }
  float l = 0.f, acc[8];
#pragma unroll
  for (int j = 0; j < 8; ++j) acc[j] = 0.f;
  float S0 = srow[4 * VIDX(0)], S1 = srow[4 * VIDX(1)], S2 = srow[4 * VIDX(2)], S3 = srow[4 * VIDX(3)];
#define CONS_V(VR, S) do { \
    const float p_ = __builtin_amdgcn_exp2f(S - M); l += p_; \
    _Pragma("unroll") for (int j_ = 0; j_ < 4; ++j_) { \
      acc[2 * j_] = fmaf(p_, bf_lo(VR[j_]), acc[2 * j_]); \
      acc[2 * j_ + 1] = fmaf(p_, bf_hi(VR[j_]), acc[2 * j_ + 1]); } \
    asm volatile("" : "+v"(acc[0]), "+v"(acc[1]), "+v"(acc[2]), "+v"(acc[3]), "+v"(acc[4]), "+v"(acc[5]), "+v"(acc[6]), "+v"(acc[7]), "+v"(l)); \
  } while (0)
#define STEP_V(it) do { \
    CONS_V(B0, S0); SCHED_FENCE(); B0 = LDV((it) + 4); S0 = srow[4 * VIDX((it) + 4)]; SCHED_FENCE(); \
    CONS_V(B1, S1); SCHED_FENCE(); B1 = LDV((it) + 5); S1 = srow[4 * VIDX((it) + 5)]; SCHED_FENCE(); \
    CONS_V(B2, S2); SCHED_FENCE(); B2 = LDV((it) + 6); S2 = srow[4 * VIDX((it) + 6)]; SCHED_FENCE(); \
    CONS_V(B3, S3); SCHED_FENCE(); B3 = LDV((it) + 7); S3 = srow[4 * VIDX((it) + 7)]; SCHED_FENCE(); } while (0)
  const int iv_w = STAG2 * (w + 1);
  {
    int it = 0;
#pragma unroll 1
    do { STEP_V(it); it += 4; } while (it < iv_w);
  }
  {
    float g = 0.f, u = 0.f;
    const int jr = min(w + NW * (lane & 7), FS - 1);
#pragma unroll
    for (int i = 0; i < NW; ++i) {
      g += lds[L_FP + i * 128 + foff + jr];
      u += lds[L_FP + i * 128 + 64 + foff + jr];
    }
    g = bfr(g); u = bfr(u);
    g = g > 0.f ? g : 0.f;
    const unsigned hb = __builtin_bit_cast(unsigned, bfr(g * u));
    float bb[8];
#pragma unroll
    for (int j = 0; j < 8; ++j) bb[j] = 0.f;
#pragma unroll
    for (int t = 0; t < DR; ++t) {
      unsigned s = __builtin_amdgcn_readlane(hb, t);
      if (w + NW * t >= FS) s = 0u;
      axpy8(bb, wdv[t], s >> 16, s);
    }
    *reinterpret_cast<f32x4*>(&lds[L_RB + w * 512 + 8 * lane]) = f32x4{bb[0], bb[1], bb[2], bb[3]};
    *reinterpret_cast<f32x4*>(&lds[L_RB + w * 512 + 8 * lane + 4]) = f32x4{bb[4], bb[5], bb[6], bb[7]};
#pragma unroll
    for (int i = 0; i < OR; i += 2) {
#pragma unroll
      for (int j = 0; j < 4; ++j) {
        const unsigned r0 = wov[i][j], r1 = wov[i + 1][j];
        wov[i][j] = __builtin_amdgcn_perm(r1, r0, 0x05040100u);
        wov[i + 1][j] = __builtin_amdgcn_perm(r1, r0, 0x07060302u);
      }
      asm volatile("" : "+v"(wov[i]), "+v"(wov[i + 1]));
    }
  }
  SCHED_FENCE();
  {
    int it = iv_w;
#pragma unroll 1
    do { STEP_V(it); it += 4; } while (it < NV - 4);
  }
  CONS_V(B0, S0); SCHED_FENCE();
  CONS_V(B1, S1); SCHED_FENCE();
  CONS_V(B2, S2); SCHED_FENCE();
  CONS_V(B3, S3);
#undef STEP_V
#undef CONS_V
#undef STEP_K
#undef CONS_K
#undef LDK
#undef LDV
#undef KIDX
#undef VIDX

  {
    l = xrow_sum(l);
#pragma unroll
    for (int j = 0; j < 8; ++j) acc[j] = xrow_sum(acc[j]);
    if (r == 0) {
      float* rec = &lds[L_REC + w * 132];
      *reinterpret_cast<f32x4*>(rec + 4 + 8 * c) = f32x4{acc[0], acc[1], acc[2], acc[3]};
      *reinterpret_cast<f32x4*>(rec + 8 + 8 * c) = f32x4{acc[4], acc[5], acc[6], acc[7]};
      if (c == 0) rec[0] = l;
    }
  }
  SCHED_FENCE();
  __syncthreads();
  SCHED_FENCE();
  {
    float L = 0.f, o = 0.f;
#pragma unroll
    for (int i = 0; i < NW; ++i) {
      L += lds[L_REC + i * 132];
      o += lds[L_REC + i * 132 + 4 + OR * w + c];
    }
    const unsigned ob = __builtin_bit_cast(unsigned, bfr(o / L));
    const unsigned nx = __builtin_amdgcn_mov_dpp(ob, 0x101  , 0xf, 0xf, true);
    const unsigned op = __builtin_amdgcn_perm(nx, ob, 0x07060302u);
    float a[8];
#pragma unroll
    for (int j = 0; j < 8; ++j) a[j] = 0.f;
#pragma unroll
    for (int i = 0; i < OR; i += 2) {
      const unsigned s = __builtin_amdgcn_readlane(op, i);
#pragma unroll
      for (int j = 0; j < 4; ++j) { a[2 * j] = dot2(wov[i][j], s, a[2 * j]); a[2 * j + 1] = dot2(wov[i + 1][j], s, a[2 * j + 1]); }
    }
    *reinterpret_cast<f32x4*>(&lds[L_RA + w * 512 + 8 * lane]) = f32x4{a[0], a[1], a[2], a[3]};
    *reinterpret_cast<f32x4*>(&lds[L_RA + w * 512 + 8 * lane + 4]) = f32x4{a[4], a[5], a[6], a[7]};
  }
  __syncthreads();
  {
    float sa = 0.f, sb = 0.f;
#pragma unroll
    for (int i = 0; i < NW; ++i) { sa += lds[L_RA + i * 512 + tid]; sb += lds[L_RB + i * 512 + tid]; }
    pa[(size_t)(b * NH + h) * (2 * DM) + tid] = sa;
    pb[(size_t)(b * NH + h) * (2 * DM) + tid] = sb;
  }
}

__global__ void __launch_bounds__(64) reduce_kernel(const uint16_t* __restrict__ x, const float* __restrict__ pp,
                                                    uint16_t* __restrict__ out) {
  const int lane = threadIdx.x, r = lane >> 4, c = lane & 15;
  const int b = blockIdx.x >> 3, n0 = (blockIdx.x & 7) * 64 + 4 * c;
  const char* base = reinterpret_cast<const char*>(pp) + (size_t)b * (NH * 2 * DM * 4);
  const unsigned voff = (unsigned)(r * 8) * (2 * DM * 4) + n0 * 4;
  f32x4 v[16];
#pragma unroll
  for (int i = 0; i < 16; ++i) v[i] = *reinterpret_cast<const f32x4*>(base + voff + i * (DM * 4));
  const u32x2 xv = *reinterpret_cast<const u32x2*>(x + b * DM + n0);
  f32x4 sa = v[0], sb = v[1];
#pragma unroll
  for (int i = 1; i < 8; ++i) { sa += v[2 * i]; sb += v[2 * i + 1]; }
  unsigned o[4];
#pragma unroll
  for (int j = 0; j < 4; ++j) {
    const float a = xrow_sum(sa[j]), bsum = xrow_sum(sb[j]);
    const float xf = (j & 1) ? bf_hi(xv[j >> 1]) : bf_lo(xv[j >> 1]);
    o[j] = __builtin_bit_cast(unsigned, bfr(bfr(xf + bfr(a)) + bfr(bsum)));
  }
  if (r == 0) *reinterpret_cast<u32x2*>(out + b * DM + n0) = u32x2{(o[0] >> 16) | (o[1] & 0xffff0000u), (o[2] >> 16) | (o[3] & 0xffff0000u)};
}

extern "C" void kernel_launch(void* const* d_in, const int* in_sizes, int n_in, void* d_out, int out_size,
                              void* d_ws, size_t ws_size, hipStream_t stream) {
  const uint16_t* x = (const uint16_t*)d_in[0];
  const uint16_t* kc = (const uint16_t*)d_in[1];
  const uint16_t* vc = (const uint16_t*)d_in[2];
  const uint16_t* wq = (const uint16_t*)d_in[3];
  const uint16_t* wo = (const uint16_t*)d_in[4];
  const uint16_t* wg = (const uint16_t*)d_in[6];
  const uint16_t* wu = (const uint16_t*)d_in[7];
  const uint16_t* wd = (const uint16_t*)d_in[8];
  float* pa = (float*)d_ws;
  float* pb = pa + DM;
  fused_decode_kernel<<<NB * NH, NT, 0, stream>>>(x, kc, vc, wq, wo, wg, wu, wd, pa, pb);
  reduce_kernel<<<NB * 8, 64, 0, stream>>>(x, pa, (uint16_t*)d_out);
}
```

```cpp
#define CFG_IL 40
#define CFG_TOUCH 16
#include <hip/hip_runtime.h>
#include <hip/hip_bf16.h>
#include <stdint.h>

typedef unsigned u32x4 __attribute__((ext_vector_type(4)));
typedef unsigned u32x2 __attribute__((ext_vector_type(2)));
typedef float f32x4 __attribute__((ext_vector_type(4)));
typedef float f32x2 __attribute__((ext_vector_type(2)));
typedef __bf16 bf16x2 __attribute__((ext_vector_type(2)));

constexpr int NB = 8, DM = 512, NH = 32, SK = 4096, DH = 128, FF = 1376, FS = 43;
constexpr int NW = 8, NT = NW * 64;
constexpr int NC = SK / 4;
constexpr int IL = CFG_IL;
#define CFG_PRE 0
constexpr int NCHT = NC - 4 * IL;
#define CFG_D 16
constexpr int NQ = NCHT / 8 + CFG_D / 2;
constexpr int NK = NCHT / 8 - CFG_D / 2;
constexpr int NV = NC / NW;
#define CFG_STAG 8
constexpr int STAG = CFG_STAG;
#define CFG_STAG2 4
constexpr int STAG2 = CFG_STAG2;
constexpr int RW = DM / NW;
constexpr int QW = 4;
constexpr int QRW = DM / QW;
constexpr int QR = QRW / 4;
constexpr int FR = RW / 8;
constexpr int OR = DH / NW;
constexpr int DR = (FS + NW - 1) / NW;
constexpr float SCALE = 0.08838834764831845f;
constexpr float LOG2E = 1.4426950408889634f;
static_assert(STAG % 4 == 0 && STAG * (NW - 1) + 16 <= NK && NQ % 4 == 0 && NK % 4 == 0 && 4 * (NQ + NK) == NCHT && RW == 64 &&
              STAG2 % 4 == 0 && STAG2 > 0 && STAG2 * NW + 8 <= NV && IL % 4 == 0, "config");

constexpr int L_XN = 0;
constexpr int L_QP = L_XN + NW * 32;
constexpr int L_QB = L_QP + NW * 128;
constexpr int L_FP = L_QB + 64;
constexpr int L_REC = L_FP + NW * 128;
constexpr int L_RA = L_REC + NW * 132;
constexpr int L_RB = L_RA + NW * 512;
constexpr int L_MX = L_RB + NW * 512;
constexpr int L_S = L_MX + NW;
constexpr int L_END = L_S + SK;

__device__ __forceinline__ float bfr(float x) { return __bfloat162float(__float2bfloat16(x)); }
__device__ __forceinline__ float bf_lo(unsigned u) { return __builtin_bit_cast(float, u << 16); }
__device__ __forceinline__ float bf_hi(unsigned u) { return __builtin_bit_cast(float, u & 0xffff0000u); }
__device__ __forceinline__ float dot2(unsigned k, unsigned q, float acc) {
  return __builtin_amdgcn_fdot2_f32_bf16(__builtin_bit_cast(bf16x2, k), __builtin_bit_cast(bf16x2, q), acc, false);
}
__device__ __forceinline__ void axpy8(float (&acc)[8], const u32x4& row, unsigned slo, unsigned shi) {
#pragma unroll
  for (int j = 0; j < 4; ++j) { acc[2 * j] = dot2(row[j], slo, acc[2 * j]); acc[2 * j + 1] = dot2(row[j], shi, acc[2 * j + 1]); }
}
template <int CTRL> __device__ __forceinline__ float dpp(float x) {
  return __builtin_bit_cast(float, __builtin_amdgcn_mov_dpp(__builtin_bit_cast(int, x), CTRL, 0xf, 0xf, true));
}
constexpr int ROR1 = 0x121, ROR2 = 0x122, ROR4 = 0x124, ROR8 = 0x128;
__device__ __forceinline__ float row16_sum(float x) {
  x += dpp<ROR8>(x); x += dpp<ROR4>(x); x += dpp<ROR2>(x); x += dpp<ROR1>(x); return x;
}
__device__ __forceinline__ float xrow_sum(float x) {
  auto s = __builtin_amdgcn_permlane16_swap(__float_as_uint(x), __float_as_uint(x), false, false);
  x = __uint_as_float(s[0]) + __uint_as_float(s[1]);
  auto t = __builtin_amdgcn_permlane32_swap(__float_as_uint(x), __float_as_uint(x), false, false);
  return __uint_as_float(t[0]) + __uint_as_float(t[1]);
}
__device__ __forceinline__ float xrow_max(float x) {
  auto s = __builtin_amdgcn_permlane16_swap(__float_as_uint(x), __float_as_uint(x), false, false);
  x = fmaxf(__uint_as_float(s[0]), __uint_as_float(s[1]));
  auto t = __builtin_amdgcn_permlane32_swap(__float_as_uint(x), __float_as_uint(x), false, false);
  return fmaxf(__uint_as_float(t[0]), __uint_as_float(t[1]));
}
__device__ __forceinline__ u32x4 ldnt(const void* p) { return __builtin_nontemporal_load(reinterpret_cast<const u32x4*>(p)); }
__device__ __forceinline__ u32x4 ld16(const void* p) { return *reinterpret_cast<const u32x4*>(p); }
__device__ __forceinline__ u32x4 ldw(const void* p) { return ld16(p); }
#define SCHED_FENCE() __builtin_amdgcn_sched_barrier(0)
#define MEM_FENCE() asm volatile("" ::: "memory")

__global__ void __launch_bounds__(NT) fused_decode_kernel(
    const uint16_t* __restrict__ x, const uint16_t* __restrict__ kc, const uint16_t* __restrict__ vc,
    const uint16_t* __restrict__ wq, const uint16_t* __restrict__ wo, const uint16_t* __restrict__ wg,
    const uint16_t* __restrict__ wu, const uint16_t* __restrict__ wd, float* __restrict__ pa,
    float* __restrict__ pb, const uint16_t* __restrict__ outp) {
  __shared__ __attribute__((aligned(16))) float lds[L_END];
  const int h = blockIdx.x & (NH - 1), b = blockIdx.x >> 5;
  const int tid = threadIdx.x, lane = tid & 63, w = __builtin_amdgcn_readfirstlane(tid >> 6);
  const int r = lane >> 4, c = lane & 15, r8 = lane >> 3, c8 = lane & 7;

  const char* kbase = reinterpret_cast<const char*>(kc) + (size_t)(b * NH + h) * (SK * DH * 2);
  const char* vbase = reinterpret_cast<const char*>(vc) + (size_t)(b * NH + h) * (SK * DH * 2);
  u32x4 KI[IL];
  const unsigned loff = lane * 16;
  u32x4 B0, B1, B2, B3;
  unsigned touch = 0;
  uint16_t* xn16 = reinterpret_cast<uint16_t*>(&lds[L_XN]);
  if (w < QW) {
    const uint16_t* xb = x + b * DM;
    const u32x4 xall = ld16(xb + 8 * lane);
    const unsigned xm0 = xb[QRW * w + lane], xm1 = xb[QRW * w + 64 + lane];
    u32x4 wqv[QR];
    SCHED_FENCE();
    {
      const char* base = reinterpret_cast<const char*>(wq) + (size_t)(QRW * w) * (NH * DH * 2) + h * (DH * 2);
      const unsigned voff = (unsigned)(QR * r) * (NH * DH * 2) + 16 * c;
#pragma unroll
      for (int i = 0; i < QR; ++i) {
        wqv[i] = ldw(base + (size_t)i * (NH * DH * 2) + voff);
        SCHED_FENCE();
      }
    }
    SCHED_FENCE();
    __builtin_amdgcn_s_barrier();
    SCHED_FENCE();
    {
      float xs[8];
#pragma unroll
      for (int j = 0; j < 4; ++j) { xs[2 * j] = bf_lo(xall[j]); xs[2 * j + 1] = bf_hi(xall[j]); }
      float s = ((xs[0] + xs[1]) + (xs[2] + xs[3])) + ((xs[4] + xs[5]) + (xs[6] + xs[7]));
      s = xrow_sum(row16_sum(s));
      const float mean = s * (1.0f / DM);
      float v = 0.f;
#pragma unroll
      for (int j = 0; j < 8; ++j) { const float d = xs[j] - mean; v = fmaf(d, d, v); }
      v = xrow_sum(row16_sum(v));
      const float var_b = bfr(v * (1.0f / DM));
      const float mean_b = bfr(mean);
      const float den = bfr(sqrtf(bfr(var_b + 1e-5f)));
      const float xn0 = bfr(bfr(bf_lo(xm0) - mean_b) / den);
      const float xn1 = bfr(bfr(bf_lo(xm1) - mean_b) / den);
      xn16[QRW * w + lane] = (uint16_t)(__builtin_bit_cast(unsigned, xn0) >> 16);
      xn16[QRW * w + 64 + lane] = (uint16_t)(__builtin_bit_cast(unsigned, xn1) >> 16);
    }
    u32x4 xa[QR / 8];
#pragma unroll
    for (int i = 0; i < QR / 8; ++i) xa[i] = *reinterpret_cast<const u32x4*>(xn16 + QRW * w + QR * r + 8 * i);
    float acc[8];
#pragma unroll
    for (int j = 0; j < 8; ++j) acc[j] = 0.f;
#pragma unroll
    for (int i = 0; i < QR; ++i) {
      const unsigned p = xa[i >> 3][(i >> 1) & 3];
      axpy8(acc, wqv[i], (i & 1) ? (p >> 16) : (p & 0xffffu), (i & 1) ? (p & 0xffff0000u) : (p << 16));
    }
#pragma unroll
    for (int j = 0; j < 8; ++j) acc[j] = xrow_sum(acc[j]);
    if (r == 0) {
      *reinterpret_cast<f32x4*>(&lds[L_QP + w * 128 + 8 * c]) = f32x4{acc[0], acc[1], acc[2], acc[3]};
      *reinterpret_cast<f32x4*>(&lds[L_QP + w * 128 + 8 * c + 4]) = f32x4{acc[4], acc[5], acc[6], acc[7]};
    }
  } else {
    const char* ki = kbase + (size_t)((w - QW) * IL) * 1024;
#pragma unroll
    for (int i = 0; i < IL; ++i) {
      if (i == CFG_PRE) { SCHED_FENCE(); __builtin_amdgcn_s_barrier(); SCHED_FENCE(); }
      KI[i] = ldnt(ki + i * 1024 + loff);
    }
  }
  SCHED_FENCE();
  __syncthreads();
  if (tid < 128) {
    float s = 0.f;
#pragma unroll
    for (int i = 0; i < QW; ++i) s += lds[L_QP + i * 128 + tid];
    reinterpret_cast<uint16_t*>(&lds[L_QB])[tid] = (uint16_t)(__builtin_bit_cast(unsigned, bfr(s)) >> 16);
  }
  SCHED_FENCE();
  __syncthreads();
#define KIDX(i) (4 * IL + ((i) < NK ? NW * (i) : (NW - QW) * NK + QW * (i)) + w)
#define VIDX(i) (NW * (i) + w)
#define LDK(i) ldnt(kbase + (size_t)KIDX(i) * 1024 + loff)
#define LDV(i) ldnt(vbase + (size_t)VIDX(i) * 1024 + loff)
  B0 = LDK(0); B1 = LDK(1); B2 = LDK(2); B3 = LDK(3);
  SCHED_FENCE();
  const u32x4 q = *reinterpret_cast<const u32x4*>(&lds[L_QB + 4 * c]);

  float mx = -INFINITY;
  float* srow = &lds[L_S + r];
#define CONS_K(KR, gc) do { \
    float d_ = dot2(KR[0], q[0], 0.f); d_ = dot2(KR[1], q[1], d_); d_ = dot2(KR[2], q[2], d_); d_ = dot2(KR[3], q[3], d_); \
    d_ = row16_sum(d_); d_ = bfr(bfr(d_) * SCALE) * LOG2E; mx = fmaxf(mx, d_); srow[4 * (gc)] = d_; \
    asm volatile("" : "+v"(mx)); } while (0)
#define STEP_K(it) do { \
    CONS_K(B0, KIDX(it));     SCHED_FENCE(); B0 = LDK((it) + 4); SCHED_FENCE(); \
    CONS_K(B1, KIDX((it) + 1)); SCHED_FENCE(); B1 = LDK((it) + 5); SCHED_FENCE(); \
    CONS_K(B2, KIDX((it) + 2)); SCHED_FENCE(); B2 = LDK((it) + 6); SCHED_FENCE(); \
    CONS_K(B3, KIDX((it) + 3)); SCHED_FENCE(); B3 = LDK((it) + 7); SCHED_FENCE(); } while (0)

  if (w >= QW) {
#pragma unroll
    for (int i = 0; i < IL; ++i) { CONS_K(KI[i], (w - QW) * IL + i); if ((i & 3) == 3) SCHED_FENCE(); }
  }
  const int it_w = STAG * w;
#pragma unroll 1
  for (int it = 0; it < it_w; it += 4) STEP_K(it);
  const int c0 = (FS * h) >> 3;
  const int foff = FS * h - 8 * c0;
  u32x4 gv[FR], uv[FR];
  {
    const unsigned voff = (unsigned)(FR * r8) * (FF * 2) + 16 * min(c0 + c8, FF / 8 - 1);
    const char* gb = reinterpret_cast<const char*>(wg) + (size_t)(RW * w) * (FF * 2);
    const char* ub = reinterpret_cast<const char*>(wu) + (size_t)(RW * w) * (FF * 2);
#pragma unroll
    for (int i = 0; i < FR; ++i) { gv[i] = ldw(gb + (size_t)i * (FF * 2) + voff); uv[i] = ldw(ub + (size_t)i * (FF * 2) + voff); }
  }
  SCHED_FENCE();
  STEP_K(it_w);
  asm volatile("" :: "v"(touch));
  {
    const u32x4 xa = *reinterpret_cast<const u32x4*>(xn16 + RW * w + FR * r8);
    float ag[8], au[8];
#pragma unroll
    for (int j = 0; j < 8; ++j) { ag[j] = 0.f; au[j] = 0.f; }
#pragma unroll
    for (int i = 0; i < FR; ++i) {
      const unsigned p = xa[i >> 1];
      const unsigned slo = (i & 1) ? (p >> 16) : (p & 0xffffu), shi = (i & 1) ? (p & 0xffff0000u) : (p << 16);
      axpy8(ag, gv[i], slo, shi);
      axpy8(au, uv[i], slo, shi);
    }
#pragma unroll
    for (int j = 0; j < 8; ++j) {
      ag[j] += dpp<ROR8>(ag[j]); ag[j] = xrow_sum(ag[j]);
      au[j] += dpp<ROR8>(au[j]); au[j] = xrow_sum(au[j]);
    }
    if (r8 == 0) {
      float* fp = &lds[L_FP + w * 128 + 8 * c8];
      *reinterpret_cast<f32x4*>(fp) = f32x4{ag[0], ag[1], ag[2], ag[3]};
      *reinterpret_cast<f32x4*>(fp + 4) = f32x4{ag[4], ag[5], ag[6], ag[7]};
      *reinterpret_cast<f32x4*>(fp + 64) = f32x4{au[0], au[1], au[2], au[3]};
      *reinterpret_cast<f32x4*>(fp + 68) = f32x4{au[4], au[5], au[6], au[7]};
    }
  }
  SCHED_FENCE();
  u32x4 wov[OR], wdv[DR];
  {
    const char* ob = reinterpret_cast<const char*>(wo) + (size_t)(h * DH + OR * w) * (DM * 2);
    const char* db = reinterpret_cast<const char*>(wd) + (size_t)(FS * h) * (DM * 2);
#pragma unroll
    for (int i = 0; i < OR; ++i) wov[i] = ldw(ob + i * (DM * 2) + loff);
#pragma unroll
    for (int i = 0; i < DR; ++i) wdv[i] = ldw(db + (size_t)min(w + NW * i, FS - 1) * (DM * 2) + loff);
  }
  SCHED_FENCE();
  {
    const int n_w = w < QW ? NQ : NK;
    STEP_K(it_w + 4);
    int it = it_w + 8;
#pragma unroll 1
    do { STEP_K(it); it += 4; } while (it < n_w - 4);
    CONS_K(B0, KIDX(it));     SCHED_FENCE(); B0 = LDV(0); SCHED_FENCE();
    CONS_K(B1, KIDX(it + 1)); SCHED_FENCE(); B1 = LDV(1); SCHED_FENCE();
    CONS_K(B2, KIDX(it + 2)); SCHED_FENCE(); B2 = LDV(2); SCHED_FENCE();
    CONS_K(B3, KIDX(it + 3)); SCHED_FENCE(); B3 = LDV(3); SCHED_FENCE();
  }
  mx = xrow_max(mx);
  if (lane == 0) lds[L_MX + w] = mx;
  MEM_FENCE();
  asm volatile("s_waitcnt lgkmcnt(0)" ::: "memory");
  __builtin_amdgcn_s_barrier();
  MEM_FENCE();
  SCHED_FENCE();
  float M;
  {
    const f32x4 m0 = *reinterpret_cast<const f32x4*>(&lds[L_MX]), m1 = *reinterpret_cast<const f32x4*>(&lds[L_MX + 4]);
    M = fmaxf(fmaxf(fmaxf(m0[0], m0[1]), fmaxf(m0[2], m0[3])), fmaxf(fmaxf(m1[0], m1[1]), fmaxf(m1[2], m1[3])));
  }
  float l = 0.f, acc[8];
#pragma unroll
  for (int j = 0; j < 8; ++j) acc[j] = 0.f;
  float S0 = srow[4 * VIDX(0)], S1 = srow[4 * VIDX(1)], S2 = srow[4 * VIDX(2)], S3 = srow[4 * VIDX(3)];
#define CONS_V(VR, S) do { \
    const float p_ = __builtin_amdgcn_exp2f(S - M); l += p_; \
    _Pragma("unroll") for (int j_ = 0; j_ < 4; ++j_) { \
      acc[2 * j_] = fmaf(p_, bf_lo(VR[j_]), acc[2 * j_]); \
      acc[2 * j_ + 1] = fmaf(p_, bf_hi(VR[j_]), acc[2 * j_ + 1]); } \
    asm volatile("" : "+v"(acc[0]), "+v"(acc[1]), "+v"(acc[2]), "+v"(acc[3]), "+v"(acc[4]), "+v"(acc[5]), "+v"(acc[6]), "+v"(acc[7]), "+v"(l)); \
  } while (0)
#define STEP_V(it) do { \
    CONS_V(B0, S0); SCHED_FENCE(); B0 = LDV((it) + 4); S0 = srow[4 * VIDX((it) + 4)]; SCHED_FENCE(); \
    CONS_V(B1, S1); SCHED_FENCE(); B1 = LDV((it) + 5); S1 = srow[4 * VIDX((it) + 5)]; SCHED_FENCE(); \
    CONS_V(B2, S2); SCHED_FENCE(); B2 = LDV((it) + 6); S2 = srow[4 * VIDX((it) + 6)]; SCHED_FENCE(); \
    CONS_V(B3, S3); SCHED_FENCE(); B3 = LDV((it) + 7); S3 = srow[4 * VIDX((it) + 7)]; SCHED_FENCE(); } while (0)
  const int iv_w = STAG2 * (w + 1);
  {
    int it = 0;
#pragma unroll 1
    do { STEP_V(it); it += 4; } while (it < iv_w);
  }
  {
    float g = 0.f, u = 0.f;
    const int jr = min(w + NW * (lane & 7), FS - 1);
#pragma unroll
    for (int i = 0; i < NW; ++i) {
      g += lds[L_FP + i * 128 + foff + jr];
      u += lds[L_FP + i * 128 + 64 + foff + jr];
    }
    g = bfr(g); u = bfr(u);
    g = g > 0.f ? g : 0.f;
    const unsigned hb = __builtin_bit_cast(unsigned, bfr(g * u));
    float bb[8];
#pragma unroll
    for (int j = 0; j < 8; ++j) bb[j] = 0.f;
#pragma unroll
    for (int t = 0; t < DR; ++t) {
      unsigned s = __builtin_amdgcn_readlane(hb, t);
      if (w + NW * t >= FS) s = 0u;
      axpy8(bb, wdv[t], s >> 16, s);
    }
    *reinterpret_cast<f32x4*>(&lds[L_RB + w * 512 + 8 * lane]) = f32x4{bb[0], bb[1], bb[2], bb[3]};
    *reinterpret_cast<f32x4*>(&lds[L_RB + w * 512 + 8 * lane + 4]) = f32x4{bb[4], bb[5], bb[6], bb[7]};
#pragma unroll
    for (int i = 0; i < OR; i += 2) {
#pragma unroll
      for (int j = 0; j < 4; ++j) {
        const unsigned r0 = wov[i][j], r1 = wov[i + 1][j];
        wov[i][j] = __builtin_amdgcn_perm(r1, r0, 0x05040100u);
        wov[i + 1][j] = __builtin_amdgcn_perm(r1, r0, 0x07060302u);
      }
      asm volatile("" : "+v"(wov[i]), "+v"(wov[i + 1]));
    }
  }
  SCHED_FENCE();
  {
    int it = iv_w;
#pragma unroll 1
    do { STEP_V(it); it += 4; } while (it < NV - 4);
  }
  CONS_V(B0, S0); SCHED_FENCE();
  CONS_V(B1, S1); SCHED_FENCE();
  CONS_V(B2, S2); SCHED_FENCE();
  CONS_V(B3, S3);
#undef STEP_V
#undef CONS_V
#undef STEP_K
#undef CONS_K
#undef LDK
#undef LDV
#undef KIDX
#undef VIDX

  {
    l = xrow_sum(l);
#pragma unroll
    for (int j = 0; j < 8; ++j) acc[j] = xrow_sum(acc[j]);
    if (r == 0) {
      float* rec = &lds[L_REC + w * 132];
      *reinterpret_cast<f32x4*>(rec + 4 + 8 * c) = f32x4{acc[0], acc[1], acc[2], acc[3]};
      *reinterpret_cast<f32x4*>(rec + 8 + 8 * c) = f32x4{acc[4], acc[5], acc[6], acc[7]};
      if (c == 0) rec[0] = l;
    }
  }
  SCHED_FENCE();
  __syncthreads();
  SCHED_FENCE();
  {
    float L = 0.f, o = 0.f;
#pragma unroll
    for (int i = 0; i < NW; ++i) {
      L += lds[L_REC + i * 132];
      o += lds[L_REC + i * 132 + 4 + OR * w + c];
    }
    const unsigned ob = __builtin_bit_cast(unsigned, bfr(o / L));
    const unsigned nx = __builtin_amdgcn_mov_dpp(ob, 0x101  , 0xf, 0xf, true);
    const unsigned op = __builtin_amdgcn_perm(nx, ob, 0x07060302u);
    float a[8];
#pragma unroll
    for (int j = 0; j < 8; ++j) a[j] = 0.f;
#pragma unroll
    for (int i = 0; i < OR; i += 2) {
      const unsigned s = __builtin_amdgcn_readlane(op, i);
#pragma unroll
      for (int j = 0; j < 4; ++j) { a[2 * j] = dot2(wov[i][j], s, a[2 * j]); a[2 * j + 1] = dot2(wov[i + 1][j], s, a[2 * j + 1]); }
    }
    *reinterpret_cast<f32x4*>(&lds[L_RA + w * 512 + 8 * lane]) = f32x4{a[0], a[1], a[2], a[3]};
    *reinterpret_cast<f32x4*>(&lds[L_RA + w * 512 + 8 * lane + 4]) = f32x4{a[4], a[5], a[6], a[7]};
  }
  __syncthreads();
  {
    float sa = 0.f, sb = 0.f;
#pragma unroll
    for (int i = 0; i < NW; ++i) { sa += lds[L_RA + i * 512 + tid]; sb += lds[L_RB + i * 512 + tid]; }
    pa[(size_t)(b * NH + h) * (2 * DM) + tid] = sa;
    pb[(size_t)(b * NH + h) * (2 * DM) + tid] = sb;
  }
}

__global__ void __launch_bounds__(64) reduce_kernel(const uint16_t* __restrict__ x, const float* __restrict__ pp,
                                                    uint16_t* __restrict__ out) {
  const int lane = threadIdx.x, r = lane >> 4, c = lane & 15;
  const int b = blockIdx.x >> 3, n0 = (blockIdx.x & 7) * 64 + 4 * c;
  const char* base = reinterpret_cast<const char*>(pp) + (size_t)b * (NH * 2 * DM * 4);
  const unsigned voff = (unsigned)(r * 8) * (2 * DM * 4) + n0 * 4;
  f32x4 v[16];
#pragma unroll
  for (int i = 0; i < 16; ++i) v[i] = *reinterpret_cast<const f32x4*>(base + voff + i * (DM * 4));
  const u32x2 xv = *reinterpret_cast<const u32x2*>(x + b * DM + n0);
  f32x4 sa = v[0], sb = v[1];
#pragma unroll
  for (int i = 1; i < 8; ++i) { sa += v[2 * i]; sb += v[2 * i + 1]; }
  unsigned o[4];
#pragma unroll
  for (int j = 0; j < 4; ++j) {
    const float a = xrow_sum(sa[j]), bsum = xrow_sum(sb[j]);
    const float xf = (j & 1) ? bf_hi(xv[j >> 1]) : bf_lo(xv[j >> 1]);
    o[j] = __builtin_bit_cast(unsigned, bfr(bfr(xf + bfr(a)) + bfr(bsum)));
  }
  if (r == 0) *reinterpret_cast<u32x2*>(out + b * DM + n0) = u32x2{(o[0] >> 16) | (o[1] & 0xffff0000u), (o[2] >> 16) | (o[3] & 0xffff0000u)};
}

extern "C" void kernel_launch(void* const* d_in, const int* in_sizes, int n_in, void* d_out, int out_size,
                              void* d_ws, size_t ws_size, hipStream_t stream) {
  const uint16_t* x = (const uint16_t*)d_in[0];
  const uint16_t* kc = (const uint16_t*)d_in[1];
  const uint16_t* vc = (const uint16_t*)d_in[2];
  const uint16_t* wq = (const uint16_t*)d_in[3];
  const uint16_t* wo = (const uint16_t*)d_in[4];
  const uint16_t* wg = (const uint16_t*)d_in[6];
  const uint16_t* wu = (const uint16_t*)d_in[7];
  const uint16_t* wd = (const uint16_t*)d_in[8];
  float* pa = (float*)d_ws;
  float* pb = pa + DM;
  fused_decode_kernel<<<NB * NH, NT, 0, stream>>>(x, kc, vc, wq, wo, wg, wu, wd, pa, pb, (const uint16_t*)d_out);
  reduce_kernel<<<NB * 8, 64, 0, stream>>>(x, pa, (uint16_t*)d_out);
}
```

```cpp
#define CFG_STAG2 12
#include <hip/hip_runtime.h>
#include <hip/hip_bf16.h>
#include <stdint.h>

typedef unsigned u32x4 __attribute__((ext_vector_type(4)));
typedef unsigned u32x2 __attribute__((ext_vector_type(2)));
typedef float f32x4 __attribute__((ext_vector_type(4)));
typedef float f32x2 __attribute__((ext_vector_type(2)));
typedef __bf16 bf16x2 __attribute__((ext_vector_type(2)));

constexpr int NB = 8, DM = 512, NH = 32, SK = 4096, DH = 128, FF = 1376, FS = 43;
constexpr int NW = 8, NT = NW * 64;
constexpr int NC = SK / 4;
#define CFG_IL 40
constexpr int IL = CFG_IL;
#define CFG_FMAP 3
#define CFG_PRE 0
constexpr int NCHT = NC - 4 * IL;
#define CFG_D 16
constexpr int NQ = NCHT / 8 + CFG_D / 2;
constexpr int NK = NCHT / 8 - CFG_D / 2;
constexpr int NV = NC / NW;
#define CFG_STAG 8
constexpr int STAG = CFG_STAG;
constexpr int STAG2 = CFG_STAG2;
constexpr int RW = DM / NW;
constexpr int QW = 4;
constexpr int QRW = DM / QW;
constexpr int QR = QRW / 4;
constexpr int FR = RW / 8;
constexpr int OR = DH / NW;
constexpr int DR = (FS + NW - 1) / NW;
#define CFG_WPS 16
constexpr int WPS = CFG_WPS;
constexpr float SCALE = 0.08838834764831845f;
constexpr float LOG2E = 1.4426950408889634f;
static_assert(STAG % 4 == 0 && STAG * (NW - 1) + 16 <= NK && NQ % 4 == 0 && NK % 4 == 0 && 4 * (NQ + NK) == NCHT && RW == 64 &&
              STAG2 % 4 == 0 && STAG2 > 0 && STAG2 * NW + 4 * (OR / WPS + 1) + 8 <= NV && IL % 4 == 0, "config");

constexpr int L_XN = 0;
constexpr int L_QP = L_XN + NW * 32;
constexpr int L_QB = L_QP + NW * 128;
constexpr int L_FP = L_QB + 64;
constexpr int L_REC = L_FP + NW * 128;
constexpr int L_RA = L_REC + NW * 132;
constexpr int L_RB = L_RA + NW * 512;
constexpr int L_MX = L_RB + NW * 512;
constexpr int L_S = L_MX + NW;
constexpr int L_END = L_S + SK;

__device__ __forceinline__ float bfr(float x) { return __bfloat162float(__float2bfloat16(x)); }
__device__ __forceinline__ float bf_lo(unsigned u) { return __builtin_bit_cast(float, u << 16); }
__device__ __forceinline__ float bf_hi(unsigned u) { return __builtin_bit_cast(float, u & 0xffff0000u); }
__device__ __forceinline__ float dot2(unsigned k, unsigned q, float acc) {
  return __builtin_amdgcn_fdot2_f32_bf16(__builtin_bit_cast(bf16x2, k), __builtin_bit_cast(bf16x2, q), acc, false);
}
__device__ __forceinline__ void axpy8(float (&acc)[8], const u32x4& row, unsigned slo, unsigned shi) {
#pragma unroll
  for (int j = 0; j < 4; ++j) { acc[2 * j] = dot2(row[j], slo, acc[2 * j]); acc[2 * j + 1] = dot2(row[j], shi, acc[2 * j + 1]); }
}
template <int CTRL> __device__ __forceinline__ float dpp(float x) {
  return __builtin_bit_cast(float, __builtin_amdgcn_mov_dpp(__builtin_bit_cast(int, x), CTRL, 0xf, 0xf, true));
}
constexpr int ROR1 = 0x121, ROR2 = 0x122, ROR4 = 0x124, ROR8 = 0x128;
__device__ __forceinline__ float row16_sum(float x) {
  x += dpp<ROR8>(x); x += dpp<ROR4>(x); x += dpp<ROR2>(x); x += dpp<ROR1>(x); return x;
}
__device__ __forceinline__ float xrow_sum(float x) {
  auto s = __builtin_amdgcn_permlane16_swap(__float_as_uint(x), __float_as_uint(x), false, false);
  x = __uint_as_float(s[0]) + __uint_as_float(s[1]);
  auto t = __builtin_amdgcn_permlane32_swap(__float_as_uint(x), __float_as_uint(x), false, false);
  return __uint_as_float(t[0]) + __uint_as_float(t[1]);
}
__device__ __forceinline__ float xrow_max(float x) {
  auto s = __builtin_amdgcn_permlane16_swap(__float_as_uint(x), __float_as_uint(x), false, false);
  x = fmaxf(__uint_as_float(s[0]), __uint_as_float(s[1]));
  auto t = __builtin_amdgcn_permlane32_swap(__float_as_uint(x), __float_as_uint(x), false, false);
  return fmaxf(__uint_as_float(t[0]), __uint_as_float(t[1]));
}
__device__ __forceinline__ u32x4 ldnt(const void* p) { return __builtin_nontemporal_load(reinterpret_cast<const u32x4*>(p)); }
__device__ __forceinline__ u32x4 ld16(const void* p) { return *reinterpret_cast<const u32x4*>(p); }
__device__ __forceinline__ u32x4 ldw(const void* p) { return ld16(p); }
#define SCHED_FENCE() __builtin_amdgcn_sched_barrier(0)
#define MEM_FENCE() asm volatile("" ::: "memory")

__global__ void __launch_bounds__(NT) fused_decode_kernel(
    const uint16_t* __restrict__ x, const uint16_t* __restrict__ kc, const uint16_t* __restrict__ vc,
    const uint16_t* __restrict__ wq, const uint16_t* __restrict__ wo, const uint16_t* __restrict__ wg,
    const uint16_t* __restrict__ wu, const uint16_t* __restrict__ wd, float* __restrict__ pa,
    float* __restrict__ pb) {
  __shared__ __attribute__((aligned(16))) float lds[L_END];
  const int h = blockIdx.x & (NH - 1), b = blockIdx.x >> 5;
  const int tid = threadIdx.x, lane = tid & 63, w = __builtin_amdgcn_readfirstlane(tid >> 6);
  const int r = lane >> 4, c = lane & 15, r8 = lane >> 3, c8 = lane & 7;

  const char* kbase = reinterpret_cast<const char*>(kc) + (size_t)(b * NH + h) * (SK * DH * 2);
  const char* vbase = reinterpret_cast<const char*>(vc) + (size_t)(b * NH + h) * (SK * DH * 2);
  u32x4 KI[IL];
  const unsigned loff = lane * 16;
  u32x4 B0, B1, B2, B3;
  uint16_t* xn16 = reinterpret_cast<uint16_t*>(&lds[L_XN]);
  if (w < QW) {
    const uint16_t* xb = x + b * DM;
    const u32x4 xall = ld16(xb + 8 * lane);
    const unsigned xm0 = xb[QRW * w + lane], xm1 = xb[QRW * w + 64 + lane];
    u32x4 wqv[QR];
    SCHED_FENCE();
    {
      const char* base = reinterpret_cast<const char*>(wq) + (size_t)(QRW * w) * (NH * DH * 2) + h * (DH * 2);
      const unsigned voff = (unsigned)(QR * r) * (NH * DH * 2) + 16 * c;
#pragma unroll
      for (int i = 0; i < QR; ++i) { wqv[i] = ldw(base + (size_t)i * (NH * DH * 2) + voff); SCHED_FENCE(); }
    }
    SCHED_FENCE();
    __builtin_amdgcn_s_barrier();
    SCHED_FENCE();
    {
      float xs[8];
#pragma unroll
      for (int j = 0; j < 4; ++j) { xs[2 * j] = bf_lo(xall[j]); xs[2 * j + 1] = bf_hi(xall[j]); }
      float s = ((xs[0] + xs[1]) + (xs[2] + xs[3])) + ((xs[4] + xs[5]) + (xs[6] + xs[7]));
      s = xrow_sum(row16_sum(s));
      const float mean = s * (1.0f / DM);
      float v = 0.f;
#pragma unroll
      for (int j = 0; j < 8; ++j) { const float d = xs[j] - mean; v = fmaf(d, d, v); }
      v = xrow_sum(row16_sum(v));
      const float var_b = bfr(v * (1.0f / DM));
      const float mean_b = bfr(mean);
      const float den = bfr(sqrtf(bfr(var_b + 1e-5f)));
      const float xn0 = bfr(bfr(bf_lo(xm0) - mean_b) / den);
      const float xn1 = bfr(bfr(bf_lo(xm1) - mean_b) / den);
      xn16[QRW * w + lane] = (uint16_t)(__builtin_bit_cast(unsigned, xn0) >> 16);
      xn16[QRW * w + 64 + lane] = (uint16_t)(__builtin_bit_cast(unsigned, xn1) >> 16);
    }
    u32x4 xa[QR / 8];
#pragma unroll
    for (int i = 0; i < QR / 8; ++i) xa[i] = *reinterpret_cast<const u32x4*>(xn16 + QRW * w + QR * r + 8 * i);
    float acc[8];
#pragma unroll
    for (int j = 0; j < 8; ++j) acc[j] = 0.f;
#pragma unroll
    for (int i = 0; i < QR; ++i) {
      const unsigned p = xa[i >> 3][(i >> 1) & 3];
      axpy8(acc, wqv[i], (i & 1) ? (p >> 16) : (p & 0xffffu), (i & 1) ? (p & 0xffff0000u) : (p << 16));
    }
#pragma unroll
    for (int j = 0; j < 8; ++j) acc[j] = xrow_sum(acc[j]);
    if (r == 0) {
      *reinterpret_cast<f32x4*>(&lds[L_QP + w * 128 + 8 * c]) = f32x4{acc[0], acc[1], acc[2], acc[3]};
      *reinterpret_cast<f32x4*>(&lds[L_QP + w * 128 + 8 * c + 4]) = f32x4{acc[4], acc[5], acc[6], acc[7]};
    }
  } else {
    const char* ki = kbase + (size_t)((w - QW) * IL) * 1024;
#pragma unroll
    for (int i = 0; i < IL; ++i) {
      if (i == CFG_PRE) { SCHED_FENCE(); __builtin_amdgcn_s_barrier(); SCHED_FENCE(); }
      KI[i] = ldnt(ki + i * 1024 + loff);
    }
  }
  SCHED_FENCE();
  __syncthreads();
  if (tid < 128) {
    float s = 0.f;
#pragma unroll
    for (int i = 0; i < QW; ++i) s += lds[L_QP + i * 128 + tid];
    reinterpret_cast<uint16_t*>(&lds[L_QB])[tid] = (uint16_t)(__builtin_bit_cast(unsigned, bfr(s)) >> 16);
  }
  SCHED_FENCE();
  __syncthreads();
#define KIDX(i) (4 * IL + ((i) < NK ? NW * (i) : (NW - QW) * NK + QW * (i)) + w)
#define VIDX(i) (NW * (i) + w)
#define LDK(i) ldnt(kbase + (size_t)KIDX(i) * 1024 + loff)
#define LDV(i) ldnt(vbase + (size_t)VIDX(i) * 1024 + loff)
  B0 = LDK(0); B1 = LDK(1); B2 = LDK(2); B3 = LDK(3);
  SCHED_FENCE();
  const u32x4 q = *reinterpret_cast<const u32x4*>(&lds[L_QB + 4 * c]);

  float mx = -INFINITY;
  float* srow = &lds[L_S + r];
#define CONS_K(KR, gc) do { \
    float d_ = dot2(KR[0], q[0], 0.f); d_ = dot2(KR[1], q[1], d_); d_ = dot2(KR[2], q[2], d_); d_ = dot2(KR[3], q[3], d_); \
    d_ = row16_sum(d_); d_ = bfr(bfr(d_) * SCALE) * LOG2E; mx = fmaxf(mx, d_); srow[4 * (gc)] = d_; \
    asm volatile("" : "+v"(mx)); } while (0)
#define STEP_K(it) do { \
    CONS_K(B0, KIDX(it));     SCHED_FENCE(); B0 = LDK((it) + 4); SCHED_FENCE(); \
    CONS_K(B1, KIDX((it) + 1)); SCHED_FENCE(); B1 = LDK((it) + 5); SCHED_FENCE(); \
    CONS_K(B2, KIDX((it) + 2)); SCHED_FENCE(); B2 = LDK((it) + 6); SCHED_FENCE(); \
    CONS_K(B3, KIDX((it) + 3)); SCHED_FENCE(); B3 = LDK((it) + 7); SCHED_FENCE(); } while (0)

  if (w >= QW) {
#pragma unroll
    for (int i = 0; i < IL; ++i) { CONS_K(KI[i], (w - QW) * IL + i); if ((i & 3) == 3) SCHED_FENCE(); }
  }
  const int it_w = STAG * w;
#pragma unroll 1
  for (int it = 0; it < it_w; it += 4) STEP_K(it);
  const int fs = FS * (4 * (h & 7) + (h >> 3));
  const int c0 = fs >> 3;
  const int foff = fs - 8 * c0;
  const int c1 = (fs + FS - 1) >> 3;
  u32x4 gv[FR], uv[FR];
  {
    const unsigned voff = (unsigned)(FR * r8) * (FF * 2) + 16 * min(c0 + c8, c1);
    const char* gb = reinterpret_cast<const char*>(wg) + (size_t)(RW * w) * (FF * 2);
    const char* ub = reinterpret_cast<const char*>(wu) + (size_t)(RW * w) * (FF * 2);
#pragma unroll
    for (int i = 0; i < FR; ++i) { gv[i] = ldw(gb + (size_t)i * (FF * 2) + voff); uv[i] = ldw(ub + (size_t)i * (FF * 2) + voff); }
  }
  SCHED_FENCE();
  STEP_K(it_w);
  {
    const u32x4 xa = *reinterpret_cast<const u32x4*>(xn16 + RW * w + FR * r8);
    float ag[8], au[8];
#pragma unroll
    for (int j = 0; j < 8; ++j) { ag[j] = 0.f; au[j] = 0.f; }
#pragma unroll
    for (int i = 0; i < FR; ++i) {
      const unsigned p = xa[i >> 1];
      const unsigned slo = (i & 1) ? (p >> 16) : (p & 0xffffu), shi = (i & 1) ? (p & 0xffff0000u) : (p << 16);
      axpy8(ag, gv[i], slo, shi);
      axpy8(au, uv[i], slo, shi);
    }
#pragma unroll
    for (int j = 0; j < 8; ++j) {
      ag[j] += dpp<ROR8>(ag[j]); ag[j] = xrow_sum(ag[j]);
      au[j] += dpp<ROR8>(au[j]); au[j] = xrow_sum(au[j]);
    }
    if (r8 == 0) {
      float* fp = &lds[L_FP + w * 128 + 8 * c8];
      *reinterpret_cast<f32x4*>(fp) = f32x4{ag[0], ag[1], ag[2], ag[3]};
      *reinterpret_cast<f32x4*>(fp + 4) = f32x4{ag[4], ag[5], ag[6], ag[7]};
      *reinterpret_cast<f32x4*>(fp + 64) = f32x4{au[0], au[1], au[2], au[3]};
      *reinterpret_cast<f32x4*>(fp + 68) = f32x4{au[4], au[5], au[6], au[7]};
    }
  }
  SCHED_FENCE();
  u32x4 wov[OR], wdv[DR];
  {
    const char* db = reinterpret_cast<const char*>(wd) + (size_t)fs * (DM * 2);
#pragma unroll
    for (int i = 0; i < DR; ++i) wdv[i] = ldw(db + (size_t)min(w + NW * i, FS - 1) * (DM * 2) + loff);
  }
  SCHED_FENCE();
  {
    const int n_w = w < QW ? NQ : NK;
    STEP_K(it_w + 4);
    int it = it_w + 8;
#pragma unroll 1
    do { STEP_K(it); it += 4; } while (it < n_w - 4);
    CONS_K(B0, KIDX(it));     SCHED_FENCE(); B0 = LDV(0); SCHED_FENCE();
    CONS_K(B1, KIDX(it + 1)); SCHED_FENCE(); B1 = LDV(1); SCHED_FENCE();
    CONS_K(B2, KIDX(it + 2)); SCHED_FENCE(); B2 = LDV(2); SCHED_FENCE();
    CONS_K(B3, KIDX(it + 3)); SCHED_FENCE(); B3 = LDV(3); SCHED_FENCE();
  }
  mx = xrow_max(mx);
  if (lane == 0) lds[L_MX + w] = mx;
  MEM_FENCE();
  asm volatile("s_waitcnt lgkmcnt(0)" ::: "memory");
  __builtin_amdgcn_s_barrier();
  MEM_FENCE();
  SCHED_FENCE();
  float M;
  {
    const f32x4 m0 = *reinterpret_cast<const f32x4*>(&lds[L_MX]), m1 = *reinterpret_cast<const f32x4*>(&lds[L_MX + 4]);
    M = fmaxf(fmaxf(fmaxf(m0[0], m0[1]), fmaxf(m0[2], m0[3])), fmaxf(fmaxf(m1[0], m1[1]), fmaxf(m1[2], m1[3])));
  }
  float l = 0.f, acc[8];
#pragma unroll
  for (int j = 0; j < 8; ++j) acc[j] = 0.f;
  float S0 = srow[4 * VIDX(0)], S1 = srow[4 * VIDX(1)], S2 = srow[4 * VIDX(2)], S3 = srow[4 * VIDX(3)];
#define CONS_V(VR, S) do { \
    const float p_ = __builtin_amdgcn_exp2f(S - M); l += p_; \
    _Pragma("unroll") for (int j_ = 0; j_ < 4; ++j_) { \
      acc[2 * j_] = fmaf(p_, bf_lo(VR[j_]), acc[2 * j_]); \
      acc[2 * j_ + 1] = fmaf(p_, bf_hi(VR[j_]), acc[2 * j_ + 1]); } \
    asm volatile("" : "+v"(acc[0]), "+v"(acc[1]), "+v"(acc[2]), "+v"(acc[3]), "+v"(acc[4]), "+v"(acc[5]), "+v"(acc[6]), "+v"(acc[7]), "+v"(l)); \
  } while (0)
#define STEP_V(it) do { \
    CONS_V(B0, S0); SCHED_FENCE(); B0 = LDV((it) + 4); S0 = srow[4 * VIDX((it) + 4)]; SCHED_FENCE(); \
    CONS_V(B1, S1); SCHED_FENCE(); B1 = LDV((it) + 5); S1 = srow[4 * VIDX((it) + 5)]; SCHED_FENCE(); \
    CONS_V(B2, S2); SCHED_FENCE(); B2 = LDV((it) + 6); S2 = srow[4 * VIDX((it) + 6)]; SCHED_FENCE(); \
    CONS_V(B3, S3); SCHED_FENCE(); B3 = LDV((it) + 7); S3 = srow[4 * VIDX((it) + 7)]; SCHED_FENCE(); } while (0)
  const int iv_w = STAG2 * (w + 1);
  {
    int it = 0;
#pragma unroll 1
    do { STEP_V(it); it += 4; } while (it < iv_w);
  }
  {
    float g = 0.f, u = 0.f;
    const int jr = min(w + NW * (lane & 7), FS - 1);
#pragma unroll
    for (int i = 0; i < NW; ++i) {
      g += lds[L_FP + i * 128 + foff + jr];
      u += lds[L_FP + i * 128 + 64 + foff + jr];
    }
    g = bfr(g); u = bfr(u);
    g = g > 0.f ? g : 0.f;
    const unsigned hb = __builtin_bit_cast(unsigned, bfr(g * u));
    float bb[8];
#pragma unroll
    for (int j = 0; j < 8; ++j) bb[j] = 0.f;
#pragma unroll
    for (int t = 0; t < DR; ++t) {
      unsigned s = __builtin_amdgcn_readlane(hb, t);
      if (w + NW * t >= FS) s = 0u;
      axpy8(bb, wdv[t], s >> 16, s);
    }
    *reinterpret_cast<f32x4*>(&lds[L_RB + w * 512 + 8 * lane]) = f32x4{bb[0], bb[1], bb[2], bb[3]};
    *reinterpret_cast<f32x4*>(&lds[L_RB + w * 512 + 8 * lane + 4]) = f32x4{bb[4], bb[5], bb[6], bb[7]};
  }
  SCHED_FENCE();
  {
    const char* ob = reinterpret_cast<const char*>(wo) + (size_t)(h * DH + OR * w) * (DM * 2);
#pragma unroll
    for (int s = 0; s < OR / WPS; ++s) {
#pragma unroll
      for (int i = 0; i < WPS; ++i) wov[s * WPS + i] = ldw(ob + (s * WPS + i) * (DM * 2) + loff);
      SCHED_FENCE();
      STEP_V(iv_w + 4 * s);
    }
    STEP_V(iv_w + 4 * (OR / WPS));
#pragma unroll
    for (int i = 0; i < OR; i += 2) {
#pragma unroll
      for (int j = 0; j < 4; ++j) {
        const unsigned r0 = wov[i][j], r1 = wov[i + 1][j];
        wov[i][j] = __builtin_amdgcn_perm(r1, r0, 0x05040100u);
        wov[i + 1][j] = __builtin_amdgcn_perm(r1, r0, 0x07060302u);
      }
      asm volatile("" : "+v"(wov[i]), "+v"(wov[i + 1]));
    }
  }
  SCHED_FENCE();
  {
    int it = iv_w + 4 * (OR / WPS + 1);
#pragma unroll 1
    do { STEP_V(it); it += 4; } while (it < NV - 4);
  }
  CONS_V(B0, S0); SCHED_FENCE();
  CONS_V(B1, S1); SCHED_FENCE();
  CONS_V(B2, S2); SCHED_FENCE();
  CONS_V(B3, S3);
#undef STEP_V
#undef CONS_V
#undef STEP_K
#undef CONS_K
#undef LDK
#undef LDV
#undef KIDX
#undef VIDX

  {
    l = xrow_sum(l);
#pragma unroll
    for (int j = 0; j < 8; ++j) acc[j] = xrow_sum(acc[j]);
    if (r == 0) {
      float* rec = &lds[L_REC + w * 132];
      *reinterpret_cast<f32x4*>(rec + 4 + 8 * c) = f32x4{acc[0], acc[1], acc[2], acc[3]};
      *reinterpret_cast<f32x4*>(rec + 8 + 8 * c) = f32x4{acc[4], acc[5], acc[6], acc[7]};
      if (c == 0) rec[0] = l;
    }
  }
  SCHED_FENCE();
  __syncthreads();
  SCHED_FENCE();
  {
    float L = 0.f, o = 0.f;
#pragma unroll
    for (int i = 0; i < NW; ++i) {
      L += lds[L_REC + i * 132];
      o += lds[L_REC + i * 132 + 4 + OR * w + c];
    }
    const unsigned ob = __builtin_bit_cast(unsigned, bfr(o / L));
    const unsigned nx = __builtin_amdgcn_mov_dpp(ob, 0x101  , 0xf, 0xf, true);
    const unsigned op = __builtin_amdgcn_perm(nx, ob, 0x07060302u);
    float a[8];
#pragma unroll
    for (int j = 0; j < 8; ++j) a[j] = 0.f;
#pragma unroll
    for (int i = 0; i < OR; i += 2) {
      const unsigned s = __builtin_amdgcn_readlane(op, i);
#pragma unroll
      for (int j = 0; j < 4; ++j) { a[2 * j] = dot2(wov[i][j], s, a[2 * j]); a[2 * j + 1] = dot2(wov[i + 1][j], s, a[2 * j + 1]); }
    }
    *reinterpret_cast<f32x4*>(&lds[L_RA + w * 512 + 8 * lane]) = f32x4{a[0], a[1], a[2], a[3]};
    *reinterpret_cast<f32x4*>(&lds[L_RA + w * 512 + 8 * lane + 4]) = f32x4{a[4], a[5], a[6], a[7]};
  }
  __syncthreads();
  {
    float sa = 0.f, sb = 0.f;
#pragma unroll
    for (int i = 0; i < NW; ++i) { sa += lds[L_RA + i * 512 + tid]; sb += lds[L_RB + i * 512 + tid]; }
    pa[(size_t)(b * NH + h) * (2 * DM) + tid] = sa;
    pb[(size_t)(b * NH + h) * (2 * DM) + tid] = sb;
  }
}

__global__ void __launch_bounds__(64) reduce_kernel(const uint16_t* __restrict__ x, const float* __restrict__ pp,
                                                    uint16_t* __restrict__ out) {
  const int lane = threadIdx.x, r = lane >> 4, c = lane & 15;
  const int b = blockIdx.x >> 3, n0 = (blockIdx.x & 7) * 64 + 4 * c;
  const char* base = reinterpret_cast<const char*>(pp) + (size_t)b * (NH * 2 * DM * 4);
  const unsigned voff = (unsigned)(r * 8) * (2 * DM * 4) + n0 * 4;
  f32x4 v[16];
#pragma unroll
  for (int i = 0; i < 16; ++i) v[i] = *reinterpret_cast<const f32x4*>(base + voff + i * (DM * 4));
  const u32x2 xv = *reinterpret_cast<const u32x2*>(x + b * DM + n0);
  f32x4 sa = v[0], sb = v[1];
#pragma unroll
  for (int i = 1; i < 8; ++i) { sa += v[2 * i]; sb += v[2 * i + 1]; }
  unsigned o[4];
#pragma unroll
  for (int j = 0; j < 4; ++j) {
    const float a = xrow_sum(sa[j]), bsum = xrow_sum(sb[j]);
    const float xf = (j & 1) ? bf_hi(xv[j >> 1]) : bf_lo(xv[j >> 1]);
    o[j] = __builtin_bit_cast(unsigned, bfr(bfr(xf + bfr(a)) + bfr(bsum)));
  }
  if (r == 0) *reinterpret_cast<u32x2*>(out + b * DM + n0) = u32x2{(o[0] >> 16) | (o[1] & 0xffff0000u), (o[2] >> 16) | (o[3] & 0xffff0000u)};
}

extern "C" void kernel_launch(void* const* d_in, const int* in_sizes, int n_in, void* d_out, int out_size,
                              void* d_ws, size_t ws_size, hipStream_t stream) {
  const uint16_t* x = (const uint16_t*)d_in[0];
  const uint16_t* kc = (const uint16_t*)d_in[1];
  const uint16_t* vc = (const uint16_t*)d_in[2];
  const uint16_t* wq = (const uint16_t*)d_in[3];
  const uint16_t* wo = (const uint16_t*)d_in[4];
  const uint16_t* wg = (const uint16_t*)d_in[6];
  const uint16_t* wu = (const uint16_t*)d_in[7];
  const uint16_t* wd = (const uint16_t*)d_in[8];
  float* pa = (float*)d_ws;
  float* pb = pa + DM;
  fused_decode_kernel<<<NB * NH, NT, 0, stream>>>(x, kc, vc, wq, wo, wg, wu, wd, pa, pb);
  reduce_kernel<<<NB * 8, 64, 0, stream>>>(x, pa, (uint16_t*)d_out);
}
```

```cpp
#define CFG_ROT 1
#define CFG_G 4
#define CFG_IL 32
#define CFG_D 32
#include <hip/hip_runtime.h>
#include <hip/hip_bf16.h>
#include <stdint.h>

typedef unsigned u32x4 __attribute__((ext_vector_type(4)));
typedef unsigned u32x2 __attribute__((ext_vector_type(2)));
typedef float f32x4 __attribute__((ext_vector_type(4)));
typedef float f32x2 __attribute__((ext_vector_type(2)));
typedef __bf16 bf16x2 __attribute__((ext_vector_type(2)));

constexpr int NB = 8, DM = 512, NH = 32, SK = 4096, DH = 128, FF = 1376, FS = 43;
constexpr int NW = 8, NT = NW * 64;
constexpr int NC = SK / 4;
constexpr int IL = CFG_IL;
#define CFG_FMAP 3
#define CFG_PRE 0
constexpr int NCHT = NC - 4 * IL;
constexpr int NQ = NCHT / 8 + CFG_D / 2;
constexpr int NK = NCHT / 8 - CFG_D / 2;
constexpr int NV = NC / NW;
#define CFG_STAG 12
constexpr int STAG = CFG_STAG;
#define CFG_STAG2 12
constexpr int STAG2 = CFG_STAG2;
constexpr int RW = DM / NW;
constexpr int QW = 4;
constexpr int QRW = DM / QW;
constexpr int QR = QRW / 4;
constexpr int FR = RW / 8;
constexpr int OR = DH / NW;
constexpr int DR = (FS + NW - 1) / NW;
#define CFG_WPS 4
constexpr int WPS = CFG_WPS;
#define CFG_GX 0
#define CFG_WD2 0
constexpr int GG = CFG_G, GX = CFG_GX;
constexpr float SCALE = 0.08838834764831845f;
constexpr float LOG2E = 1.4426950408889634f;
static_assert(STAG % 4 == 0 && STAG * (NW - 1) + 4 * (GG + GX + 1) + 8 <= (CFG_ROT ? NQ : NK) && STAG * (QW - 1) + 4 * (GG + GX + 1) + 8 <= NK && NQ % 4 == 0 && NK % 4 == 0 && 4 * (NQ + NK) == NCHT && RW == 64 &&
              STAG2 % 4 == 0 && STAG2 > 0 && STAG2 * NW + 4 * (OR / WPS + 1 + CFG_WD2) + 8 <= NV && IL % 4 == 0, "config");

constexpr int L_XN = 0;
constexpr int L_QP = L_XN + NW * 32;
constexpr int L_QB = L_QP + NW * 128;
constexpr int L_FP = L_QB + 64;
constexpr int L_REC = L_FP + NW * 128;
constexpr int L_RA = L_REC + NW * 132;
constexpr int L_RB = L_RA + NW * 512;
constexpr int L_MX = L_RB + NW * 512;
constexpr int L_S = L_MX + NW;
constexpr int L_END = L_S + SK;

__device__ __forceinline__ float bfr(float x) { return __bfloat162float(__float2bfloat16(x)); }
__device__ __forceinline__ float bf_lo(unsigned u) { return __builtin_bit_cast(float, u << 16); }
__device__ __forceinline__ float bf_hi(unsigned u) { return __builtin_bit_cast(float, u & 0xffff0000u); }
__device__ __forceinline__ float dot2(unsigned k, unsigned q, float acc) {
  return __builtin_amdgcn_fdot2_f32_bf16(__builtin_bit_cast(bf16x2, k), __builtin_bit_cast(bf16x2, q), acc, false);
}
__device__ __forceinline__ void axpy8(float (&acc)[8], const u32x4& row, unsigned slo, unsigned shi) {
#pragma unroll
  for (int j = 0; j < 4; ++j) { acc[2 * j] = dot2(row[j], slo, acc[2 * j]); acc[2 * j + 1] = dot2(row[j], shi, acc[2 * j + 1]); }
}
template <int CTRL> __device__ __forceinline__ float dpp(float x) {
  return __builtin_bit_cast(float, __builtin_amdgcn_mov_dpp(__builtin_bit_cast(int, x), CTRL, 0xf, 0xf, true));
}
constexpr int ROR1 = 0x121, ROR2 = 0x122, ROR4 = 0x124, ROR8 = 0x128;
__device__ __forceinline__ float row16_sum(float x) {
  x += dpp<ROR8>(x); x += dpp<ROR4>(x); x += dpp<ROR2>(x); x += dpp<ROR1>(x); return x;
}
__device__ __forceinline__ float xrow_sum(float x) {
  auto s = __builtin_amdgcn_permlane16_swap(__float_as_uint(x), __float_as_uint(x), false, false);
  x = __uint_as_float(s[0]) + __uint_as_float(s[1]);
  auto t = __builtin_amdgcn_permlane32_swap(__float_as_uint(x), __float_as_uint(x), false, false);
  return __uint_as_float(t[0]) + __uint_as_float(t[1]);
}
__device__ __forceinline__ float xrow_max(float x) {
  auto s = __builtin_amdgcn_permlane16_swap(__float_as_uint(x), __float_as_uint(x), false, false);
  x = fmaxf(__uint_as_float(s[0]), __uint_as_float(s[1]));
  auto t = __builtin_amdgcn_permlane32_swap(__float_as_uint(x), __float_as_uint(x), false, false);
  return fmaxf(__uint_as_float(t[0]), __uint_as_float(t[1]));
}
__device__ __forceinline__ u32x4 ldnt(const void* p) { return __builtin_nontemporal_load(reinterpret_cast<const u32x4*>(p)); }
__device__ __forceinline__ u32x4 ld16(const void* p) { return *reinterpret_cast<const u32x4*>(p); }
__device__ __forceinline__ u32x4 ldw(const void* p) { return ld16(p); }
#define SCHED_FENCE() __builtin_amdgcn_sched_barrier(0)
#define MEM_FENCE() asm volatile("" ::: "memory")

__global__ void __launch_bounds__(NT) fused_decode_kernel(
    const uint16_t* __restrict__ x, const uint16_t* __restrict__ kc, const uint16_t* __restrict__ vc,
    const uint16_t* __restrict__ wq, const uint16_t* __restrict__ wo, const uint16_t* __restrict__ wg,
    const uint16_t* __restrict__ wu, const uint16_t* __restrict__ wd, float* __restrict__ pa,
    float* __restrict__ pb) {
  __shared__ __attribute__((aligned(16))) float lds[L_END];
  const int h = blockIdx.x & (NH - 1), b = blockIdx.x >> 5;
  const int tid = threadIdx.x, lane = tid & 63, w = __builtin_amdgcn_readfirstlane(tid >> 6);
  const int r = lane >> 4, c = lane & 15, r8 = lane >> 3, c8 = lane & 7;

  const char* kbase = reinterpret_cast<const char*>(kc) + (size_t)(b * NH + h) * (SK * DH * 2);
  const char* vbase = reinterpret_cast<const char*>(vc) + (size_t)(b * NH + h) * (SK * DH * 2);
  u32x4 KI[IL];
  const unsigned loff = lane * 16;
  u32x4 B0, B1, B2, B3;
  uint16_t* xn16 = reinterpret_cast<uint16_t*>(&lds[L_XN]);
  if (w < QW) {
    const uint16_t* xb = x + b * DM;
    const u32x4 xall = ld16(xb + 8 * lane);
    const unsigned xm0 = xb[QRW * w + lane], xm1 = xb[QRW * w + 64 + lane];
    u32x4 wqv[QR];
    SCHED_FENCE();
    {
      const char* base = reinterpret_cast<const char*>(wq) + (size_t)(QRW * w) * (NH * DH * 2) + h * (DH * 2);
      const unsigned voff = (unsigned)(QR * r) * (NH * DH * 2) + 16 * c;
#pragma unroll
      for (int i = 0; i < QR; ++i) { wqv[i] = ldw(base + (size_t)i * (NH * DH * 2) + voff); SCHED_FENCE(); }
    }
    SCHED_FENCE();
    __builtin_amdgcn_s_barrier();
    SCHED_FENCE();
    {
      float xs[8];
#pragma unroll
      for (int j = 0; j < 4; ++j) { xs[2 * j] = bf_lo(xall[j]); xs[2 * j + 1] = bf_hi(xall[j]); }
      float s = ((xs[0] + xs[1]) + (xs[2] + xs[3])) + ((xs[4] + xs[5]) + (xs[6] + xs[7]));
      s = xrow_sum(row16_sum(s));
      const float mean = s * (1.0f / DM);
      float v = 0.f;
#pragma unroll
      for (int j = 0; j < 8; ++j) { const float d = xs[j] - mean; v = fmaf(d, d, v); }
      v = xrow_sum(row16_sum(v));
      const float var_b = bfr(v * (1.0f / DM));
      const float mean_b = bfr(mean);
      const float den = bfr(sqrtf(bfr(var_b + 1e-5f)));
      const float xn0 = bfr(bfr(bf_lo(xm0) - mean_b) / den);
      const float xn1 = bfr(bfr(bf_lo(xm1) - mean_b) / den);
      xn16[QRW * w + lane] = (uint16_t)(__builtin_bit_cast(unsigned, xn0) >> 16);
      xn16[QRW * w + 64 + lane] = (uint16_t)(__builtin_bit_cast(unsigned, xn1) >> 16);
    }
    u32x4 xa[QR / 8];
#pragma unroll
    for (int i = 0; i < QR / 8; ++i) xa[i] = *reinterpret_cast<const u32x4*>(xn16 + QRW * w + QR * r + 8 * i);
    float acc[8];
#pragma unroll
    for (int j = 0; j < 8; ++j) acc[j] = 0.f;
#pragma unroll
    for (int i = 0; i < QR; ++i) {
      const unsigned p = xa[i >> 3][(i >> 1) & 3];
      axpy8(acc, wqv[i], (i & 1) ? (p >> 16) : (p & 0xffffu), (i & 1) ? (p & 0xffff0000u) : (p << 16));
    }
#pragma unroll
    for (int j = 0; j < 8; ++j) acc[j] = xrow_sum(acc[j]);
    if (r == 0) {
      *reinterpret_cast<f32x4*>(&lds[L_QP + w * 128 + 8 * c]) = f32x4{acc[0], acc[1], acc[2], acc[3]};
      *reinterpret_cast<f32x4*>(&lds[L_QP + w * 128 + 8 * c + 4]) = f32x4{acc[4], acc[5], acc[6], acc[7]};
    }
  } else {
    const char* ki = kbase + (size_t)((w - QW) * IL) * 1024;
#pragma unroll
    for (int i = 0; i < IL; ++i) {
      if (i == CFG_PRE) { SCHED_FENCE(); __builtin_amdgcn_s_barrier(); SCHED_FENCE(); }
      KI[i] = ldnt(ki + i * 1024 + loff);
    }
  }
  SCHED_FENCE();
  __syncthreads();
  if (tid < 128) {
    float s = 0.f;
#pragma unroll
    for (int i = 0; i < QW; ++i) s += lds[L_QP + i * 128 + tid];
    reinterpret_cast<uint16_t*>(&lds[L_QB])[tid] = (uint16_t)(__builtin_bit_cast(unsigned, bfr(s)) >> 16);
  }
  SCHED_FENCE();
  __syncthreads();
#define KIDX(i) (4 * IL + ((i) < NK ? NW * (i) : (NW - QW) * NK + QW * (i)) + w)
#define VIDX(i) (NW * (i) + w)
#define LDK(i) ldnt(kbase + (size_t)KIDX(i) * 1024 + loff)
#define LDV(i) ldnt(vbase + (size_t)VIDX(i) * 1024 + loff)
  B0 = LDK(0); B1 = LDK(1); B2 = LDK(2); B3 = LDK(3);
  SCHED_FENCE();
  const u32x4 q = *reinterpret_cast<const u32x4*>(&lds[L_QB + 4 * c]);

  float mx = -INFINITY;
  float* srow = &lds[L_S + r];
#define CONS_K(KR, gc) do { \
    float d_ = dot2(KR[0], q[0], 0.f); d_ = dot2(KR[1], q[1], d_); d_ = dot2(KR[2], q[2], d_); d_ = dot2(KR[3], q[3], d_); \
    d_ = row16_sum(d_); d_ = bfr(bfr(d_) * SCALE) * LOG2E; mx = fmaxf(mx, d_); srow[4 * (gc)] = d_; \
    asm volatile("" : "+v"(mx)); } while (0)
#define STEP_K(it) do { \
    CONS_K(B0, KIDX(it));     SCHED_FENCE(); B0 = LDK((it) + 4); SCHED_FENCE(); \
    CONS_K(B1, KIDX((it) + 1)); SCHED_FENCE(); B1 = LDK((it) + 5); SCHED_FENCE(); \
    CONS_K(B2, KIDX((it) + 2)); SCHED_FENCE(); B2 = LDK((it) + 6); SCHED_FENCE(); \
    CONS_K(B3, KIDX((it) + 3)); SCHED_FENCE(); B3 = LDK((it) + 7); SCHED_FENCE(); } while (0)

  if (w >= QW) {
#pragma unroll
    for (int i = 0; i < IL; ++i) { CONS_K(KI[i], (w - QW) * IL + i); if ((i & 3) == 3) SCHED_FENCE(); }
  }
  const int it_w = STAG * (CFG_ROT ? (w + QW) & (NW - 1) : w);
#pragma unroll 1
  for (int it = 0; it < it_w; it += 4) STEP_K(it);
  const int fs = FS * (4 * (h & 7) + (h >> 3));
  const int c0 = fs >> 3;
  const int foff = fs - 8 * c0;
  const int c1 = (fs + FS - 1) >> 3;
  u32x4 gv[FR], uv[FR];
  {
    const unsigned voff = (unsigned)(FR * r8) * (FF * 2) + 16 * min(c0 + c8, c1);
    const char* gb = reinterpret_cast<const char*>(wg) + (size_t)(RW * w) * (FF * 2);
    const char* ub = reinterpret_cast<const char*>(wu) + (size_t)(RW * w) * (FF * 2);
#pragma unroll
    for (int s = 0; s < GG; ++s) {
#pragma unroll
      for (int i = s * (FR / GG); i < (s + 1) * (FR / GG); ++i) { gv[i] = ldw(gb + (size_t)i * (FF * 2) + voff); uv[i] = ldw(ub + (size_t)i * (FF * 2) + voff); }
      SCHED_FENCE();
      STEP_K(it_w + 4 * s);
    }
#pragma unroll
    for (int s = 0; s < GX; ++s) STEP_K(it_w + 4 * (GG + s));
  }
  {
    const u32x4 xa = *reinterpret_cast<const u32x4*>(xn16 + RW * w + FR * r8);
    float ag[8], au[8];
#pragma unroll
    for (int j = 0; j < 8; ++j) { ag[j] = 0.f; au[j] = 0.f; }
#pragma unroll
    for (int i = 0; i < FR; ++i) {
      const unsigned p = xa[i >> 1];
      const unsigned slo = (i & 1) ? (p >> 16) : (p & 0xffffu), shi = (i & 1) ? (p & 0xffff0000u) : (p << 16);
      axpy8(ag, gv[i], slo, shi);
      axpy8(au, uv[i], slo, shi);
    }
#pragma unroll
    for (int j = 0; j < 8; ++j) {
      ag[j] += dpp<ROR8>(ag[j]); ag[j] = xrow_sum(ag[j]);
      au[j] += dpp<ROR8>(au[j]); au[j] = xrow_sum(au[j]);
    }
    if (r8 == 0) {
      float* fp = &lds[L_FP + w * 128 + 8 * c8];
      *reinterpret_cast<f32x4*>(fp) = f32x4{ag[0], ag[1], ag[2], ag[3]};
      *reinterpret_cast<f32x4*>(fp + 4) = f32x4{ag[4], ag[5], ag[6], ag[7]};
      *reinterpret_cast<f32x4*>(fp + 64) = f32x4{au[0], au[1], au[2], au[3]};
      *reinterpret_cast<f32x4*>(fp + 68) = f32x4{au[4], au[5], au[6], au[7]};
    }
  }
  SCHED_FENCE();
  u32x4 wov[OR], wdv[DR];
  const char* db = reinterpret_cast<const char*>(wd) + (size_t)fs * (DM * 2);
#pragma unroll
  for (int i = 0; i < DR; ++i) wdv[i] = ldw(db + (size_t)min(w + NW * i, FS - 1) * (DM * 2) + loff);
  SCHED_FENCE();
  {
    const int n_w = w < QW ? NQ : NK;
    STEP_K(it_w + 4 * (GG + GX));
    int it = it_w + 4 * (GG + GX + 1);
#pragma unroll 1
    do { STEP_K(it); it += 4; } while (it < n_w - 4);
    CONS_K(B0, KIDX(it));     SCHED_FENCE(); B0 = LDV(0); SCHED_FENCE();
    CONS_K(B1, KIDX(it + 1)); SCHED_FENCE(); B1 = LDV(1); SCHED_FENCE();
    CONS_K(B2, KIDX(it + 2)); SCHED_FENCE(); B2 = LDV(2); SCHED_FENCE();
    CONS_K(B3, KIDX(it + 3)); SCHED_FENCE(); B3 = LDV(3); SCHED_FENCE();
  }
  mx = xrow_max(mx);
  if (lane == 0) lds[L_MX + w] = mx;
  MEM_FENCE();
  asm volatile("s_waitcnt lgkmcnt(0)" ::: "memory");
  __builtin_amdgcn_s_barrier();
  MEM_FENCE();
  SCHED_FENCE();
  float M;
  {
    const f32x4 m0 = *reinterpret_cast<const f32x4*>(&lds[L_MX]), m1 = *reinterpret_cast<const f32x4*>(&lds[L_MX + 4]);
    M = fmaxf(fmaxf(fmaxf(m0[0], m0[1]), fmaxf(m0[2], m0[3])), fmaxf(fmaxf(m1[0], m1[1]), fmaxf(m1[2], m1[3])));
  }
  float l = 0.f, acc[8];
#pragma unroll
  for (int j = 0; j < 8; ++j) acc[j] = 0.f;
  float S0 = srow[4 * VIDX(0)], S1 = srow[4 * VIDX(1)], S2 = srow[4 * VIDX(2)], S3 = srow[4 * VIDX(3)];
#define CONS_V(VR, S) do { \
    const float p_ = __builtin_amdgcn_exp2f(S - M); l += p_; \
    _Pragma("unroll") for (int j_ = 0; j_ < 4; ++j_) { \
      acc[2 * j_] = fmaf(p_, bf_lo(VR[j_]), acc[2 * j_]); \
      acc[2 * j_ + 1] = fmaf(p_, bf_hi(VR[j_]), acc[2 * j_ + 1]); } \
    asm volatile("" : "+v"(acc[0]), "+v"(acc[1]), "+v"(acc[2]), "+v"(acc[3]), "+v"(acc[4]), "+v"(acc[5]), "+v"(acc[6]), "+v"(acc[7]), "+v"(l)); \
  } while (0)
#define STEP_V(it) do { \
    CONS_V(B0, S0); SCHED_FENCE(); B0 = LDV((it) + 4); S0 = srow[4 * VIDX((it) + 4)]; SCHED_FENCE(); \
    CONS_V(B1, S1); SCHED_FENCE(); B1 = LDV((it) + 5); S1 = srow[4 * VIDX((it) + 5)]; SCHED_FENCE(); \
    CONS_V(B2, S2); SCHED_FENCE(); B2 = LDV((it) + 6); S2 = srow[4 * VIDX((it) + 6)]; SCHED_FENCE(); \
    CONS_V(B3, S3); SCHED_FENCE(); B3 = LDV((it) + 7); S3 = srow[4 * VIDX((it) + 7)]; SCHED_FENCE(); } while (0)
  const int iv_0 = STAG2 * (w + 1), iv_w = iv_0 + 4 * CFG_WD2;
  {
    int it = 0;
#pragma unroll 1
    do { STEP_V(it); it += 4; } while (it < iv_0);
  }
  {
    float g = 0.f, u = 0.f;
    const int jr = min(w + NW * (lane & 7), FS - 1);
#pragma unroll
    for (int i = 0; i < NW; ++i) {
      g += lds[L_FP + i * 128 + foff + jr];
      u += lds[L_FP + i * 128 + 64 + foff + jr];
    }
    g = bfr(g); u = bfr(u);
    g = g > 0.f ? g : 0.f;
    const unsigned hb = __builtin_bit_cast(unsigned, bfr(g * u));
    float bb[8];
#pragma unroll
    for (int j = 0; j < 8; ++j) bb[j] = 0.f;
#pragma unroll
    for (int t = 0; t < DR; ++t) {
      unsigned s = __builtin_amdgcn_readlane(hb, t);
      if (w + NW * t >= FS) s = 0u;
      axpy8(bb, wdv[t], s >> 16, s);
    }
    *reinterpret_cast<f32x4*>(&lds[L_RB + w * 512 + 8 * lane]) = f32x4{bb[0], bb[1], bb[2], bb[3]};
    *reinterpret_cast<f32x4*>(&lds[L_RB + w * 512 + 8 * lane + 4]) = f32x4{bb[4], bb[5], bb[6], bb[7]};
  }
  SCHED_FENCE();
  {
    const char* ob = reinterpret_cast<const char*>(wo) + (size_t)(h * DH + OR * w) * (DM * 2);
#pragma unroll
    for (int s = 0; s < OR / WPS; ++s) {
#pragma unroll
      for (int i = 0; i < WPS; ++i) wov[s * WPS + i] = ldw(ob + (s * WPS + i) * (DM * 2) + loff);
      SCHED_FENCE();
      STEP_V(iv_w + 4 * s);
    }
    STEP_V(iv_w + 4 * (OR / WPS));
#pragma unroll
    for (int i = 0; i < OR; i += 2) {
#pragma unroll
      for (int j = 0; j < 4; ++j) {
        const unsigned r0 = wov[i][j], r1 = wov[i + 1][j];
        wov[i][j] = __builtin_amdgcn_perm(r1, r0, 0x05040100u);
        wov[i + 1][j] = __builtin_amdgcn_perm(r1, r0, 0x07060302u);
      }
      asm volatile("" : "+v"(wov[i]), "+v"(wov[i + 1]));
    }
  }
  SCHED_FENCE();
  {
    int it = iv_w + 4 * (OR / WPS + 1);
#pragma unroll 1
    do { STEP_V(it); it += 4; } while (it < NV - 4);
  }
  CONS_V(B0, S0); SCHED_FENCE();
  CONS_V(B1, S1); SCHED_FENCE();
  CONS_V(B2, S2); SCHED_FENCE();
  CONS_V(B3, S3);
#undef STEP_V
#undef CONS_V
#undef STEP_K
#undef CONS_K
#undef LDK
#undef LDV
#undef KIDX
#undef VIDX

  {
    l = xrow_sum(l);
#pragma unroll
    for (int j = 0; j < 8; ++j) acc[j] = xrow_sum(acc[j]);
    if (r == 0) {
      float* rec = &lds[L_REC + w * 132];
      *reinterpret_cast<f32x4*>(rec + 4 + 8 * c) = f32x4{acc[0], acc[1], acc[2], acc[3]};
      *reinterpret_cast<f32x4*>(rec + 8 + 8 * c) = f32x4{acc[4], acc[5], acc[6], acc[7]};
      if (c == 0) rec[0] = l;
    }
  }
  SCHED_FENCE();
  __syncthreads();
  SCHED_FENCE();
  {
    float L = 0.f, o = 0.f;
#pragma unroll
    for (int i = 0; i < NW; ++i) {
      L += lds[L_REC + i * 132];
      o += lds[L_REC + i * 132 + 4 + OR * w + c];
    }
    const unsigned ob = __builtin_bit_cast(unsigned, bfr(o / L));
    const unsigned nx = __builtin_amdgcn_mov_dpp(ob, 0x101  , 0xf, 0xf, true);
    const unsigned op = __builtin_amdgcn_perm(nx, ob, 0x07060302u);
    float a[8];
#pragma unroll
    for (int j = 0; j < 8; ++j) a[j] = 0.f;
#pragma unroll
    for (int i = 0; i < OR; i += 2) {
      const unsigned s = __builtin_amdgcn_readlane(op, i);
#pragma unroll
      for (int j = 0; j < 4; ++j) { a[2 * j] = dot2(wov[i][j], s, a[2 * j]); a[2 * j + 1] = dot2(wov[i + 1][j], s, a[2 * j + 1]); }
    }
    *reinterpret_cast<f32x4*>(&lds[L_RA + w * 512 + 8 * lane]) = f32x4{a[0], a[1], a[2], a[3]};
    *reinterpret_cast<f32x4*>(&lds[L_RA + w * 512 + 8 * lane + 4]) = f32x4{a[4], a[5], a[6], a[7]};
  }
  __syncthreads();
  {
    float sa = 0.f, sb = 0.f;
#pragma unroll
    for (int i = 0; i < NW; ++i) { sa += lds[L_RA + i * 512 + tid]; sb += lds[L_RB + i * 512 + tid]; }
    pa[(size_t)(b * NH + h) * (2 * DM) + tid] = sa;
    pb[(size_t)(b * NH + h) * (2 * DM) + tid] = sb;
  }
}

__global__ void __launch_bounds__(64) reduce_kernel(const uint16_t* __restrict__ x, const float* __restrict__ pp,
                                                    uint16_t* __restrict__ out) {
  const int lane = threadIdx.x, r = lane >> 4, c = lane & 15;
  const int b = blockIdx.x >> 3, n0 = (blockIdx.x & 7) * 64 + 4 * c;
  const char* base = reinterpret_cast<const char*>(pp) + (size_t)b * (NH * 2 * DM * 4);
  const unsigned voff = (unsigned)(r * 8) * (2 * DM * 4) + n0 * 4;
  f32x4 v[16];
#pragma unroll
  for (int i = 0; i < 16; ++i) v[i] = *reinterpret_cast<const f32x4*>(base + voff + i * (DM * 4));
  const u32x2 xv = *reinterpret_cast<const u32x2*>(x + b * DM + n0);
  f32x4 sa = v[0], sb = v[1];
#pragma unroll
  for (int i = 1; i < 8; ++i) { sa += v[2 * i]; sb += v[2 * i + 1]; }
  unsigned o[4];
#pragma unroll
  for (int j = 0; j < 4; ++j) {
    const float a = xrow_sum(sa[j]), bsum = xrow_sum(sb[j]);
    const float xf = (j & 1) ? bf_hi(xv[j >> 1]) : bf_lo(xv[j >> 1]);
    o[j] = __builtin_bit_cast(unsigned, bfr(bfr(xf + bfr(a)) + bfr(bsum)));
  }
  if (r == 0) *reinterpret_cast<u32x2*>(out + b * DM + n0) = u32x2{(o[0] >> 16) | (o[1] & 0xffff0000u), (o[2] >> 16) | (o[3] & 0xffff0000u)};
}

extern "C" void kernel_launch(void* const* d_in, const int* in_sizes, int n_in, void* d_out, int out_size,
                              void* d_ws, size_t ws_size, hipStream_t stream) {
  const uint16_t* x = (const uint16_t*)d_in[0];
  const uint16_t* kc = (const uint16_t*)d_in[1];
  const uint16_t* vc = (const uint16_t*)d_in[2];
  const uint16_t* wq = (const uint16_t*)d_in[3];
  const uint16_t* wo = (const uint16_t*)d_in[4];
  const uint16_t* wg = (const uint16_t*)d_in[6];
  const uint16_t* wu = (const uint16_t*)d_in[7];
  const uint16_t* wd = (const uint16_t*)d_in[8];
  float* pa = (float*)d_ws;
  float* pb = pa + DM;
  fused_decode_kernel<<<NB * NH, NT, 0, stream>>>(x, kc, vc, wq, wo, wg, wu, wd, pa, pb);
  reduce_kernel<<<NB * 8, 64, 0, stream>>>(x, pa, (uint16_t*)d_out);
}
```

```cpp
#define W_NT 1
#include <hip/hip_runtime.h>
#include <hip/hip_bf16.h>
#include <stdint.h>

typedef unsigned u32x4 __attribute__((ext_vector_type(4)));
typedef unsigned u32x2 __attribute__((ext_vector_type(2)));
typedef float f32x4 __attribute__((ext_vector_type(4)));
typedef float f32x2 __attribute__((ext_vector_type(2)));
typedef __bf16 bf16x2 __attribute__((ext_vector_type(2)));

constexpr int NB = 8, DM = 512, NH = 32, SK = 4096, DH = 128, FF = 1376, FS = 43;
constexpr int NW = 8, NT = NW * 64;
constexpr int NC = SK / 4;
#define CFG_IL 32
constexpr int IL = CFG_IL;
#define CFG_FMAP 3
#define CFG_PRE 0
constexpr int NCHT = NC - 4 * IL;
#define CFG_D 32
constexpr int NQ = NCHT / 8 + CFG_D / 2;
constexpr int NK = NCHT / 8 - CFG_D / 2;
constexpr int NV = NC / NW;
#define CFG_STAG 12
constexpr int STAG = CFG_STAG;
#define CFG_STAG2 12
constexpr int STAG2 = CFG_STAG2;
constexpr int RW = DM / NW;
constexpr int QW = 4;
constexpr int QRW = DM / QW;
constexpr int QR = QRW / 4;
constexpr int FR = RW / 8;
constexpr int OR = DH / NW;
constexpr int DR = (FS + NW - 1) / NW;
#define CFG_WPS 4
constexpr int WPS = CFG_WPS;
#define CFG_II 1
#define CFG_G 4
#define CFG_GX 0
#define CFG_WD2 0
#define CFG_ROT 1
constexpr int GG = CFG_G, GX = CFG_GX;
constexpr float SCALE = 0.08838834764831845f;
constexpr float LOG2E = 1.4426950408889634f;
static_assert(STAG % 4 == 0 && STAG * (NW - 1) + 4 * (GG + GX + 1) + 8 <= (CFG_ROT ? NQ : NK) && STAG * (QW - 1) + 4 * (GG + GX + 1) + 8 <= NK && NQ % 4 == 0 && NK % 4 == 0 && 4 * (NQ + NK) == NCHT && RW == 64 &&
              STAG2 % 4 == 0 && STAG2 > 0 && STAG2 * NW + 4 * (OR / WPS + 1 + CFG_WD2) + 8 <= NV && IL % 4 == 0, "config");

constexpr int L_XN = 0;
constexpr int L_QP = L_XN + NW * 32;
constexpr int L_QB = L_QP + NW * 128;
constexpr int L_FP = L_QB + 64;
constexpr int L_REC = L_FP + NW * 128;
constexpr int L_RA = L_REC + NW * 132;
constexpr int L_RB = L_RA + NW * 512;
constexpr int L_MX = L_RB + NW * 512;
constexpr int L_S = L_MX + NW;
constexpr int L_END = L_S + SK;

__device__ __forceinline__ float bfr(float x) { return __bfloat162float(__float2bfloat16(x)); }
__device__ __forceinline__ float bf_lo(unsigned u) { return __builtin_bit_cast(float, u << 16); }
__device__ __forceinline__ float bf_hi(unsigned u) { return __builtin_bit_cast(float, u & 0xffff0000u); }
__device__ __forceinline__ float dot2(unsigned k, unsigned q, float acc) {
  return __builtin_amdgcn_fdot2_f32_bf16(__builtin_bit_cast(bf16x2, k), __builtin_bit_cast(bf16x2, q), acc, false);
}
__device__ __forceinline__ void axpy8(float (&acc)[8], const u32x4& row, unsigned slo, unsigned shi) {
#pragma unroll
  for (int j = 0; j < 4; ++j) { acc[2 * j] = dot2(row[j], slo, acc[2 * j]); acc[2 * j + 1] = dot2(row[j], shi, acc[2 * j + 1]); }
}
template <int CTRL> __device__ __forceinline__ float dpp(float x) {
  return __builtin_bit_cast(float, __builtin_amdgcn_mov_dpp(__builtin_bit_cast(int, x), CTRL, 0xf, 0xf, true));
}
constexpr int ROR1 = 0x121, ROR2 = 0x122, ROR4 = 0x124, ROR8 = 0x128;
__device__ __forceinline__ float row16_sum(float x) {
  x += dpp<ROR8>(x); x += dpp<ROR4>(x); x += dpp<ROR2>(x); x += dpp<ROR1>(x); return x;
}
__device__ __forceinline__ float xrow_sum(float x) {
  auto s = __builtin_amdgcn_permlane16_swap(__float_as_uint(x), __float_as_uint(x), false, false);
  x = __uint_as_float(s[0]) + __uint_as_float(s[1]);
  auto t = __builtin_amdgcn_permlane32_swap(__float_as_uint(x), __float_as_uint(x), false, false);
  return __uint_as_float(t[0]) + __uint_as_float(t[1]);
}
__device__ __forceinline__ float xrow_max(float x) {
  auto s = __builtin_amdgcn_permlane16_swap(__float_as_uint(x), __float_as_uint(x), false, false);
  x = fmaxf(__uint_as_float(s[0]), __uint_as_float(s[1]));
  auto t = __builtin_amdgcn_permlane32_swap(__float_as_uint(x), __float_as_uint(x), false, false);
  return fmaxf(__uint_as_float(t[0]), __uint_as_float(t[1]));
}
__device__ __forceinline__ u32x4 ldnt(const void* p) { return __builtin_nontemporal_load(reinterpret_cast<const u32x4*>(p)); }
__device__ __forceinline__ u32x4 ld16(const void* p) { return *reinterpret_cast<const u32x4*>(p); }
template <int BIT> __device__ __forceinline__ u32x4 ldw(const void* p) { return (W_NT & BIT) ? ldnt(p) : ld16(p); }
#define SCHED_FENCE() __builtin_amdgcn_sched_barrier(0)
#define MEM_FENCE() asm volatile("" ::: "memory")

__global__ void __launch_bounds__(NT) fused_decode_kernel(
    const uint16_t* __restrict__ x, const uint16_t* __restrict__ kc, const uint16_t* __restrict__ vc,
    const uint16_t* __restrict__ wq, const uint16_t* __restrict__ wo, const uint16_t* __restrict__ wg,
    const uint16_t* __restrict__ wu, const uint16_t* __restrict__ wd, float* __restrict__ pa,
    float* __restrict__ pb) {
  __shared__ __attribute__((aligned(16))) float lds[L_END];
  const int h = blockIdx.x & (NH - 1), b = blockIdx.x >> 5;
  const int tid = threadIdx.x, lane = tid & 63, w = __builtin_amdgcn_readfirstlane(tid >> 6);
  const int r = lane >> 4, c = lane & 15, r8 = lane >> 3, c8 = lane & 7;

  const char* kbase = reinterpret_cast<const char*>(kc) + (size_t)(b * NH + h) * (SK * DH * 2);
  const char* vbase = reinterpret_cast<const char*>(vc) + (size_t)(b * NH + h) * (SK * DH * 2);
  u32x4 KI[IL];
  const unsigned loff = lane * 16;
  u32x4 B0, B1, B2, B3;
  uint16_t* xn16 = reinterpret_cast<uint16_t*>(&lds[L_XN]);
  if (w < QW) {
    const uint16_t* xb = x + b * DM;
    const u32x4 xall = ld16(xb + 8 * lane);
    const unsigned xm0 = xb[QRW * w + lane], xm1 = xb[QRW * w + 64 + lane];
    u32x4 wqv[QR];
    SCHED_FENCE();
    {
      const char* base = reinterpret_cast<const char*>(wq) + (size_t)(QRW * w) * (NH * DH * 2) + h * (DH * 2);
      const unsigned voff = (unsigned)(QR * r) * (NH * DH * 2) + 16 * c;
#pragma unroll
      for (int i = 0; i < QR; ++i) { wqv[i] = ldw<1>(base + (size_t)i * (NH * DH * 2) + voff); SCHED_FENCE(); }
    }
    SCHED_FENCE();
    __builtin_amdgcn_s_barrier();
    SCHED_FENCE();
    {
      float xs[8];
#pragma unroll
      for (int j = 0; j < 4; ++j) { xs[2 * j] = bf_lo(xall[j]); xs[2 * j + 1] = bf_hi(xall[j]); }
      float s = ((xs[0] + xs[1]) + (xs[2] + xs[3])) + ((xs[4] + xs[5]) + (xs[6] + xs[7]));
      s = xrow_sum(row16_sum(s));
      const float mean = s * (1.0f / DM);
      float v = 0.f;
#pragma unroll
      for (int j = 0; j < 8; ++j) { const float d = xs[j] - mean; v = fmaf(d, d, v); }
      v = xrow_sum(row16_sum(v));
      const float var_b = bfr(v * (1.0f / DM));
      const float mean_b = bfr(mean);
      const float den = bfr(sqrtf(bfr(var_b + 1e-5f)));
      const float xn0 = bfr(bfr(bf_lo(xm0) - mean_b) / den);
      const float xn1 = bfr(bfr(bf_lo(xm1) - mean_b) / den);
      xn16[QRW * w + lane] = (uint16_t)(__builtin_bit_cast(unsigned, xn0) >> 16);
      xn16[QRW * w + 64 + lane] = (uint16_t)(__builtin_bit_cast(unsigned, xn1) >> 16);
    }
    u32x4 xa[QR / 8];
#pragma unroll
    for (int i = 0; i < QR / 8; ++i) xa[i] = *reinterpret_cast<const u32x4*>(xn16 + QRW * w + QR * r + 8 * i);
    float acc[8];
#pragma unroll
    for (int j = 0; j < 8; ++j) acc[j] = 0.f;
#pragma unroll
    for (int i = 0; i < QR; ++i) {
      const unsigned p = xa[i >> 3][(i >> 1) & 3];
      axpy8(acc, wqv[i], (i & 1) ? (p >> 16) : (p & 0xffffu), (i & 1) ? (p & 0xffff0000u) : (p << 16));
    }
#pragma unroll
    for (int j = 0; j < 8; ++j) acc[j] = xrow_sum(acc[j]);
    if (r == 0) {
      *reinterpret_cast<f32x4*>(&lds[L_QP + w * 128 + 8 * c]) = f32x4{acc[0], acc[1], acc[2], acc[3]};
      *reinterpret_cast<f32x4*>(&lds[L_QP + w * 128 + 8 * c + 4]) = f32x4{acc[4], acc[5], acc[6], acc[7]};
    }
  } else {
    const char* ki = kbase + (size_t)((w - QW) * (CFG_II ? 1 : IL)) * 1024;
#pragma unroll
    for (int i = 0; i < IL; ++i) {
      if (i == CFG_PRE) { SCHED_FENCE(); __builtin_amdgcn_s_barrier(); SCHED_FENCE(); }
      KI[i] = ldnt(ki + i * (CFG_II ? 4096 : 1024) + loff);
    }
  }
  SCHED_FENCE();
  __syncthreads();
  if (tid < 128) {
    float s = 0.f;
#pragma unroll
    for (int i = 0; i < QW; ++i) s += lds[L_QP + i * 128 + tid];
    reinterpret_cast<uint16_t*>(&lds[L_QB])[tid] = (uint16_t)(__builtin_bit_cast(unsigned, bfr(s)) >> 16);
  }
  SCHED_FENCE();
  __syncthreads();
#define KIDX(i) (4 * IL + ((i) < NK ? NW * (i) : (NW - QW) * NK + QW * (i)) + w)
#define VIDX(i) (NW * (i) + w)
#define LDK(i) ldnt(kbase + (size_t)KIDX(i) * 1024 + loff)
#define LDV(i) ldnt(vbase + (size_t)VIDX(i) * 1024 + loff)
  B0 = LDK(0); B1 = LDK(1); B2 = LDK(2); B3 = LDK(3);
  SCHED_FENCE();
  const u32x4 q = *reinterpret_cast<const u32x4*>(&lds[L_QB + 4 * c]);

  float mx = -INFINITY;
  float* srow = &lds[L_S + r];
#define CONS_K(KR, gc) do { \
    float d_ = dot2(KR[0], q[0], 0.f); d_ = dot2(KR[1], q[1], d_); d_ = dot2(KR[2], q[2], d_); d_ = dot2(KR[3], q[3], d_); \
    d_ = row16_sum(d_); d_ = bfr(bfr(d_) * SCALE) * LOG2E; mx = fmaxf(mx, d_); srow[4 * (gc)] = d_; \
    asm volatile("" : "+v"(mx)); } while (0)
#define STEP_K(it) do { \
    CONS_K(B0, KIDX(it));     SCHED_FENCE(); B0 = LDK((it) + 4); SCHED_FENCE(); \
    CONS_K(B1, KIDX((it) + 1)); SCHED_FENCE(); B1 = LDK((it) + 5); SCHED_FENCE(); \
    CONS_K(B2, KIDX((it) + 2)); SCHED_FENCE(); B2 = LDK((it) + 6); SCHED_FENCE(); \
    CONS_K(B3, KIDX((it) + 3)); SCHED_FENCE(); B3 = LDK((it) + 7); SCHED_FENCE(); } while (0)

  if (w >= QW) {
#pragma unroll
    for (int i = 0; i < IL; ++i) { CONS_K(KI[i], CFG_II ? 4 * i + (w - QW) : (w - QW) * IL + i); if ((i & 3) == 3) SCHED_FENCE(); }
  }
  const int it_w = STAG * (CFG_ROT ? (w + QW) & (NW - 1) : w);
#pragma unroll 1
  for (int it = 0; it < it_w; it += 4) STEP_K(it);
  const int fs = FS * (4 * (h & 7) + (h >> 3));
  const int c0 = fs >> 3;
  const int foff = fs - 8 * c0;
  const int c1 = (fs + FS - 1) >> 3;
  u32x4 gv[FR], uv[FR];
  {
    const unsigned voff = (unsigned)(FR * r8) * (FF * 2) + 16 * min(c0 + c8, c1);
    const char* gb = reinterpret_cast<const char*>(wg) + (size_t)(RW * w) * (FF * 2);
    const char* ub = reinterpret_cast<const char*>(wu) + (size_t)(RW * w) * (FF * 2);
#pragma unroll
    for (int s = 0; s < GG; ++s) {
#pragma unroll
      for (int i = s * (FR / GG); i < (s + 1) * (FR / GG); ++i) { gv[i] = ldw<2>(gb + (size_t)i * (FF * 2) + voff); uv[i] = ldw<2>(ub + (size_t)i * (FF * 2) + voff); }
      SCHED_FENCE();
      STEP_K(it_w + 4 * s);
    }
#pragma unroll
    for (int s = 0; s < GX; ++s) STEP_K(it_w + 4 * (GG + s));
  }
  {
    const u32x4 xa = *reinterpret_cast<const u32x4*>(xn16 + RW * w + FR * r8);
    float ag[8], au[8];
#pragma unroll
    for (int j = 0; j < 8; ++j) { ag[j] = 0.f; au[j] = 0.f; }
#pragma unroll
    for (int i = 0; i < FR; ++i) {
      const unsigned p = xa[i >> 1];
      const unsigned slo = (i & 1) ? (p >> 16) : (p & 0xffffu), shi = (i & 1) ? (p & 0xffff0000u) : (p << 16);
      axpy8(ag, gv[i], slo, shi);
      axpy8(au, uv[i], slo, shi);
    }
#pragma unroll
    for (int j = 0; j < 8; ++j) {
      ag[j] += dpp<ROR8>(ag[j]); ag[j] = xrow_sum(ag[j]);
      au[j] += dpp<ROR8>(au[j]); au[j] = xrow_sum(au[j]);
    }
    if (r8 == 0) {
      float* fp = &lds[L_FP + w * 128 + 8 * c8];
      *reinterpret_cast<f32x4*>(fp) = f32x4{ag[0], ag[1], ag[2], ag[3]};
      *reinterpret_cast<f32x4*>(fp + 4) = f32x4{ag[4], ag[5], ag[6], ag[7]};
      *reinterpret_cast<f32x4*>(fp + 64) = f32x4{au[0], au[1], au[2], au[3]};
      *reinterpret_cast<f32x4*>(fp + 68) = f32x4{au[4], au[5], au[6], au[7]};
    }
  }
  SCHED_FENCE();
  u32x4 wov[OR], wdv[DR];
  const char* db = reinterpret_cast<const char*>(wd) + (size_t)fs * (DM * 2);
#pragma unroll
  for (int i = 0; i < DR; ++i) wdv[i] = ldw<4>(db + (size_t)min(w + NW * i, FS - 1) * (DM * 2) + loff);
  SCHED_FENCE();
  {
    const int n_w = w < QW ? NQ : NK;
    STEP_K(it_w + 4 * (GG + GX));
    int it = it_w + 4 * (GG + GX + 1);
#pragma unroll 1
    do { STEP_K(it); it += 4; } while (it < n_w - 4);
    CONS_K(B0, KIDX(it));     SCHED_FENCE(); B0 = LDV(0); SCHED_FENCE();
    CONS_K(B1, KIDX(it + 1)); SCHED_FENCE(); B1 = LDV(1); SCHED_FENCE();
    CONS_K(B2, KIDX(it + 2)); SCHED_FENCE(); B2 = LDV(2); SCHED_FENCE();
    CONS_K(B3, KIDX(it + 3)); SCHED_FENCE(); B3 = LDV(3); SCHED_FENCE();
  }
  mx = xrow_max(mx);
  if (lane == 0) lds[L_MX + w] = mx;
  MEM_FENCE();
  asm volatile("s_waitcnt lgkmcnt(0)" ::: "memory");
  __builtin_amdgcn_s_barrier();
  MEM_FENCE();
  SCHED_FENCE();
  float M;
  {
    const f32x4 m0 = *reinterpret_cast<const f32x4*>(&lds[L_MX]), m1 = *reinterpret_cast<const f32x4*>(&lds[L_MX + 4]);
    M = fmaxf(fmaxf(fmaxf(m0[0], m0[1]), fmaxf(m0[2], m0[3])), fmaxf(fmaxf(m1[0], m1[1]), fmaxf(m1[2], m1[3])));
  }
  float l = 0.f, acc[8];
#pragma unroll
  for (int j = 0; j < 8; ++j) acc[j] = 0.f;
  float S0 = srow[4 * VIDX(0)], S1 = srow[4 * VIDX(1)], S2 = srow[4 * VIDX(2)], S3 = srow[4 * VIDX(3)];
#define CONS_V(VR, S) do { \
    const float p_ = __builtin_amdgcn_exp2f(S - M); l += p_; \
    _Pragma("unroll") for (int j_ = 0; j_ < 4; ++j_) { \
      acc[2 * j_] = fmaf(p_, bf_lo(VR[j_]), acc[2 * j_]); \
      acc[2 * j_ + 1] = fmaf(p_, bf_hi(VR[j_]), acc[2 * j_ + 1]); } \
    asm volatile("" : "+v"(acc[0]), "+v"(acc[1]), "+v"(acc[2]), "+v"(acc[3]), "+v"(acc[4]), "+v"(acc[5]), "+v"(acc[6]), "+v"(acc[7]), "+v"(l)); \
  } while (0)
#define STEP_V(it) do { \
    CONS_V(B0, S0); SCHED_FENCE(); B0 = LDV((it) + 4); S0 = srow[4 * VIDX((it) + 4)]; SCHED_FENCE(); \
    CONS_V(B1, S1); SCHED_FENCE(); B1 = LDV((it) + 5); S1 = srow[4 * VIDX((it) + 5)]; SCHED_FENCE(); \
    CONS_V(B2, S2); SCHED_FENCE(); B2 = LDV((it) + 6); S2 = srow[4 * VIDX((it) + 6)]; SCHED_FENCE(); \
    CONS_V(B3, S3); SCHED_FENCE(); B3 = LDV((it) + 7); S3 = srow[4 * VIDX((it) + 7)]; SCHED_FENCE(); } while (0)
  const int iv_0 = STAG2 * (w + 1), iv_w = iv_0 + 4 * CFG_WD2;
  {
    int it = 0;
#pragma unroll 1
    do { STEP_V(it); it += 4; } while (it < iv_0);
  }
  {
    float g = 0.f, u = 0.f;
    const int jr = min(w + NW * (lane & 7), FS - 1);
#pragma unroll
    for (int i = 0; i < NW; ++i) {
      g += lds[L_FP + i * 128 + foff + jr];
      u += lds[L_FP + i * 128 + 64 + foff + jr];
    }
    g = bfr(g); u = bfr(u);
    g = g > 0.f ? g : 0.f;
    const unsigned hb = __builtin_bit_cast(unsigned, bfr(g * u));
    float bb[8];
#pragma unroll
    for (int j = 0; j < 8; ++j) bb[j] = 0.f;
#pragma unroll
    for (int t = 0; t < DR; ++t) {
      unsigned s = __builtin_amdgcn_readlane(hb, t);
      if (w + NW * t >= FS) s = 0u;
      axpy8(bb, wdv[t], s >> 16, s);
    }
    *reinterpret_cast<f32x4*>(&lds[L_RB + w * 512 + 8 * lane]) = f32x4{bb[0], bb[1], bb[2], bb[3]};
    *reinterpret_cast<f32x4*>(&lds[L_RB + w * 512 + 8 * lane + 4]) = f32x4{bb[4], bb[5], bb[6], bb[7]};
  }
  SCHED_FENCE();
  {
    const char* ob = reinterpret_cast<const char*>(wo) + (size_t)(h * DH + OR * w) * (DM * 2);
#pragma unroll
    for (int s = 0; s < OR / WPS; ++s) {
#pragma unroll
      for (int i = 0; i < WPS; ++i) wov[s * WPS + i] = ldw<8>(ob + (s * WPS + i) * (DM * 2) + loff);
      SCHED_FENCE();
      STEP_V(iv_w + 4 * s);
    }
    STEP_V(iv_w + 4 * (OR / WPS));
#pragma unroll
    for (int i = 0; i < OR; i += 2) {
#pragma unroll
      for (int j = 0; j < 4; ++j) {
        const unsigned r0 = wov[i][j], r1 = wov[i + 1][j];
        wov[i][j] = __builtin_amdgcn_perm(r1, r0, 0x05040100u);
        wov[i + 1][j] = __builtin_amdgcn_perm(r1, r0, 0x07060302u);
      }
      asm volatile("" : "+v"(wov[i]), "+v"(wov[i + 1]));
    }
  }
  SCHED_FENCE();
  {
    int it = iv_w + 4 * (OR / WPS + 1);
#pragma unroll 1
    do { STEP_V(it); it += 4; } while (it < NV - 4);
  }
  CONS_V(B0, S0); SCHED_FENCE();
  CONS_V(B1, S1); SCHED_FENCE();
  CONS_V(B2, S2); SCHED_FENCE();
  CONS_V(B3, S3);
#undef STEP_V
#undef CONS_V
#undef STEP_K
#undef CONS_K
#undef LDK
#undef LDV
#undef KIDX
#undef VIDX

  {
    l = xrow_sum(l);
#pragma unroll
    for (int j = 0; j < 8; ++j) acc[j] = xrow_sum(acc[j]);
    if (r == 0) {
      float* rec = &lds[L_REC + w * 132];
      *reinterpret_cast<f32x4*>(rec + 4 + 8 * c) = f32x4{acc[0], acc[1], acc[2], acc[3]};
      *reinterpret_cast<f32x4*>(rec + 8 + 8 * c) = f32x4{acc[4], acc[5], acc[6], acc[7]};
      if (c == 0) rec[0] = l;
    }
  }
  SCHED_FENCE();
  __syncthreads();
  SCHED_FENCE();
  {
    float L = 0.f, o = 0.f;
#pragma unroll
    for (int i = 0; i < NW; ++i) {
      L += lds[L_REC + i * 132];
      o += lds[L_REC + i * 132 + 4 + OR * w + c];
    }
    const unsigned ob = __builtin_bit_cast(unsigned, bfr(o / L));
    const unsigned nx = __builtin_amdgcn_mov_dpp(ob, 0x101  , 0xf, 0xf, true);
    const unsigned op = __builtin_amdgcn_perm(nx, ob, 0x07060302u);
    float a[8];
#pragma unroll
    for (int j = 0; j < 8; ++j) a[j] = 0.f;
#pragma unroll
    for (int i = 0; i < OR; i += 2) {
      const unsigned s = __builtin_amdgcn_readlane(op, i);
#pragma unroll
      for (int j = 0; j < 4; ++j) { a[2 * j] = dot2(wov[i][j], s, a[2 * j]); a[2 * j + 1] = dot2(wov[i + 1][j], s, a[2 * j + 1]); }
    }
    *reinterpret_cast<f32x4*>(&lds[L_RA + w * 512 + 8 * lane]) = f32x4{a[0], a[1], a[2], a[3]};
    *reinterpret_cast<f32x4*>(&lds[L_RA + w * 512 + 8 * lane + 4]) = f32x4{a[4], a[5], a[6], a[7]};
  }
  __syncthreads();
  {
    float sa = 0.f, sb = 0.f;
#pragma unroll
    for (int i = 0; i < NW; ++i) { sa += lds[L_RA + i * 512 + tid]; sb += lds[L_RB + i * 512 + tid]; }
    pa[(size_t)(b * NH + h) * (2 * DM) + tid] = sa;
    pb[(size_t)(b * NH + h) * (2 * DM) + tid] = sb;
  }
}

__global__ void __launch_bounds__(64) reduce_kernel(const uint16_t* __restrict__ x, const float* __restrict__ pp,
                                                    uint16_t* __restrict__ out) {
  const int lane = threadIdx.x, r = lane >> 4, c = lane & 15;
  const int b = blockIdx.x >> 3, n0 = (blockIdx.x & 7) * 64 + 4 * c;
  const char* base = reinterpret_cast<const char*>(pp) + (size_t)b * (NH * 2 * DM * 4);
  const unsigned voff = (unsigned)(r * 8) * (2 * DM * 4) + n0 * 4;
  f32x4 v[16];
#pragma unroll
  for (int i = 0; i < 16; ++i) v[i] = *reinterpret_cast<const f32x4*>(base + voff + i * (DM * 4));
  const u32x2 xv = *reinterpret_cast<const u32x2*>(x + b * DM + n0);
  f32x4 sa = v[0], sb = v[1];
#pragma unroll
  for (int i = 1; i < 8; ++i) { sa += v[2 * i]; sb += v[2 * i + 1]; }
  unsigned o[4];
#pragma unroll
  for (int j = 0; j < 4; ++j) {
    const float a = xrow_sum(sa[j]), bsum = xrow_sum(sb[j]);
    const float xf = (j & 1) ? bf_hi(xv[j >> 1]) : bf_lo(xv[j >> 1]);
    o[j] = __builtin_bit_cast(unsigned, bfr(bfr(xf + bfr(a)) + bfr(bsum)));
  }
  if (r == 0) *reinterpret_cast<u32x2*>(out + b * DM + n0) = u32x2{(o[0] >> 16) | (o[1] & 0xffff0000u), (o[2] >> 16) | (o[3] & 0xffff0000u)};
}

extern "C" void kernel_launch(void* const* d_in, const int* in_sizes, int n_in, void* d_out, int out_size,
                              void* d_ws, size_t ws_size, hipStream_t stream) {
  const uint16_t* x = (const uint16_t*)d_in[0];
  const uint16_t* kc = (const uint16_t*)d_in[1];
  const uint16_t* vc = (const uint16_t*)d_in[2];
  const uint16_t* wq = (const uint16_t*)d_in[3];
  const uint16_t* wo = (const uint16_t*)d_in[4];
  const uint16_t* wg = (const uint16_t*)d_in[6];
  const uint16_t* wu = (const uint16_t*)d_in[7];
  const uint16_t* wd = (const uint16_t*)d_in[8];
  float* pa = (float*)d_ws;
  float* pb = pa + DM;
  fused_decode_kernel<<<NB * NH, NT, 0, stream>>>(x, kc, vc, wq, wo, wg, wu, wd, pa, pb);
  reduce_kernel<<<NB * 8, 64, 0, stream>>>(x, pa, (uint16_t*)d_out);
}
```

```cpp
#include <hip/hip_runtime.h>
#include <hip/hip_bf16.h>
#include <stdint.h>

typedef unsigned u32x4 __attribute__((ext_vector_type(4)));
typedef unsigned u32x2 __attribute__((ext_vector_type(2)));
typedef float f32x4 __attribute__((ext_vector_type(4)));
typedef float f32x2 __attribute__((ext_vector_type(2)));
typedef __bf16 bf16x2 __attribute__((ext_vector_type(2)));

constexpr int NB = 8, DM = 512, NH = 32, SK = 4096, DH = 128, FF = 1376, FS = 43;
constexpr int NW = 8, NT = NW * 64;
constexpr int NC = SK / 4;
#define CFG_IL 32
constexpr int IL = CFG_IL;
#define CFG_FMAP 3
#define CFG_PW 16
#define CFG_SAUX 16
#define CFG_LAUX 2
#define CFG_Q1 8
#define CFG_QROT 1
#define CFG_PRE 8
constexpr int NCHT = NC - 4 * IL;
#define CFG_D 32
constexpr int NQ = NCHT / 8 + CFG_D / 2;
constexpr int NK = NCHT / 8 - CFG_D / 2;
constexpr int NV = NC / NW;
#define CFG_STAG 12
constexpr int STAG = CFG_STAG;
#define CFG_STAG2 12
constexpr int STAG2 = CFG_STAG2;
constexpr int RW = DM / NW;
constexpr int QW = 4;
constexpr int QRW = DM / QW;
constexpr int QR = QRW / 4;
constexpr int FR = RW / 8;
constexpr int OR = DH / NW;
constexpr int DR = (FS + NW - 1) / NW;
#define CFG_WPS 4
constexpr int WPS = CFG_WPS;
#define CFG_II 1
#define CFG_G 4
#define CFG_GX 0
#define CFG_ROT 1
constexpr int GG = CFG_G, GX = CFG_GX;
constexpr float SCALE = 0.08838834764831845f;
constexpr float LOG2E = 1.4426950408889634f;
static_assert(STAG % 4 == 0 && STAG * (NW - 1) + 4 * (GG + GX + 1) + 8 <= (CFG_ROT ? NQ : NK) && STAG * (QW - 1) + 4 * (GG + GX + 1) + 8 <= NK && NQ % 4 == 0 && NK % 4 == 0 && 4 * (NQ + NK) == NCHT && RW == 64 &&
              STAG2 % 4 == 0 && STAG2 > 0 && STAG2 * NW + 4 * (OR / WPS + 1 + 0) + 8 <= NV && IL % 4 == 0, "config");

constexpr int L_XN = 0;
constexpr int L_QP = L_XN + NW * 32;
constexpr int L_QB = L_QP + NW * 128;
constexpr int L_FP = L_QB + 64;
constexpr int L_REC = L_FP + NW * 128;
constexpr int L_RA = L_REC + (1 ? 4 : 1) * NW * 132;
constexpr int L_RB = L_RA + NW * 512;
constexpr int L_MX = L_RB + NW * 512;
constexpr int L_S = L_MX + NW;
constexpr int L_END = L_S + SK;

__device__ __forceinline__ float bfr(float x) { return __bfloat162float(__float2bfloat16(x)); }
__device__ __forceinline__ float bf_lo(unsigned u) { return __builtin_bit_cast(float, u << 16); }
__device__ __forceinline__ float bf_hi(unsigned u) { return __builtin_bit_cast(float, u & 0xffff0000u); }
__device__ __forceinline__ float dot2(unsigned k, unsigned q, float acc) {
  return __builtin_amdgcn_fdot2_f32_bf16(__builtin_bit_cast(bf16x2, k), __builtin_bit_cast(bf16x2, q), acc, false);
}
__device__ __forceinline__ void axpy8(float (&acc)[8], const u32x4& row, unsigned slo, unsigned shi) {
#pragma unroll
  for (int j = 0; j < 4; ++j) { acc[2 * j] = dot2(row[j], slo, acc[2 * j]); acc[2 * j + 1] = dot2(row[j], shi, acc[2 * j + 1]); }
}
template <int CTRL> __device__ __forceinline__ float dpp(float x) {
  return __builtin_bit_cast(float, __builtin_amdgcn_mov_dpp(__builtin_bit_cast(int, x), CTRL, 0xf, 0xf, true));
}
constexpr int ROR1 = 0x121, ROR2 = 0x122, ROR4 = 0x124, ROR8 = 0x128;
__device__ __forceinline__ float row16_sum(float x) {
  x += dpp<ROR8>(x); x += dpp<ROR4>(x); x += dpp<ROR2>(x); x += dpp<ROR1>(x); return x;
}
__device__ __forceinline__ float xrow_sum(float x) {
  auto s = __builtin_amdgcn_permlane16_swap(__float_as_uint(x), __float_as_uint(x), false, false);
  x = __uint_as_float(s[0]) + __uint_as_float(s[1]);
  auto t = __builtin_amdgcn_permlane32_swap(__float_as_uint(x), __float_as_uint(x), false, false);
  return __uint_as_float(t[0]) + __uint_as_float(t[1]);
}
__device__ __forceinline__ float sum8(const float (&v)[8]) {
  const f32x2 p = (f32x2{v[0], v[1]} + f32x2{v[2], v[3]}) + (f32x2{v[4], v[5]} + f32x2{v[6], v[7]});
  return p[0] + p[1];
}
__device__ __forceinline__ float xrow_max(float x) {
  auto s = __builtin_amdgcn_permlane16_swap(__float_as_uint(x), __float_as_uint(x), false, false);
  x = fmaxf(__uint_as_float(s[0]), __uint_as_float(s[1]));
  auto t = __builtin_amdgcn_permlane32_swap(__float_as_uint(x), __float_as_uint(x), false, false);
  return fmaxf(__uint_as_float(t[0]), __uint_as_float(t[1]));
}
__device__ __forceinline__ u32x4 ldnt(const void* p) { return __builtin_nontemporal_load(reinterpret_cast<const u32x4*>(p)); }
__device__ __forceinline__ u32x4 ld16(const void* p) { return *reinterpret_cast<const u32x4*>(p); }
#define W_NT 0
template <int BIT> __device__ __forceinline__ u32x4 ldw(const void* p) { return (W_NT & BIT) ? ldnt(p) : ld16(p); }
#define SCHED_FENCE() __builtin_amdgcn_sched_barrier(0)
#define MEM_FENCE() asm volatile("" ::: "memory")

__global__ void __launch_bounds__(NT) fused_decode_kernel(
    const uint16_t* __restrict__ x, const uint16_t* __restrict__ kc, const uint16_t* __restrict__ vc,
    const uint16_t* __restrict__ wq, const uint16_t* __restrict__ wo, const uint16_t* __restrict__ wg,
    const uint16_t* __restrict__ wu, const uint16_t* __restrict__ wd, float* __restrict__ pa,
    float* __restrict__ pb) {
  __shared__ __attribute__((aligned(16))) float lds[L_END];
  const int h = blockIdx.x & (NH - 1), b = blockIdx.x >> 5;
  const int tid = threadIdx.x, lane = tid & 63, w = __builtin_amdgcn_readfirstlane(tid >> 6);
  const int r = lane >> 4, c = lane & 15, r8 = lane >> 3, c8 = lane & 7;

  const char* kbase = reinterpret_cast<const char*>(kc) + (size_t)(b * NH + h) * (SK * DH * 2);
  const char* vbase = reinterpret_cast<const char*>(vc) + (size_t)(b * NH + h) * (SK * DH * 2);
  u32x4 KI[IL];
  const unsigned loff = lane * 16;
  u32x4 B0, B1, B2, B3;
  uint16_t* xn16 = reinterpret_cast<uint16_t*>(&lds[L_XN]);
  if (w < QW) {
    const uint16_t* xb = x + b * DM;
    const u32x4 xall = ld16(xb + 8 * lane);
    const unsigned xm0 = xb[QRW * w + lane], xm1 = xb[QRW * w + 64 + lane];
    u32x4 wqv[QR];
    SCHED_FENCE();
    {
      const char* base = reinterpret_cast<const char*>(wq) + (size_t)(QRW * w) * (NH * DH * 2) + h * (DH * 2);
      const unsigned voff = (unsigned)(QR * r) * (NH * DH * 2) + 16 * c;
#pragma unroll
      for (int i = 0; i < QR; ++i) {
        const int ri = CFG_QROT ? 4 * (((i >> 2) + b) & 7) + (i & 3) : i;
        if (CFG_Q1 && i == CFG_Q1) { asm volatile("s_waitcnt vmcnt(0)"); SCHED_FENCE(); }
        wqv[i] = ldw<1>(base + (size_t)ri * (NH * DH * 2) + voff); SCHED_FENCE();
      }
    }
    SCHED_FENCE();
    __builtin_amdgcn_s_barrier();
    SCHED_FENCE();
    {
      float xs[8];
#pragma unroll
      for (int j = 0; j < 4; ++j) { xs[2 * j] = bf_lo(xall[j]); xs[2 * j + 1] = bf_hi(xall[j]); }
      float s = ((xs[0] + xs[1]) + (xs[2] + xs[3])) + ((xs[4] + xs[5]) + (xs[6] + xs[7]));
      s = xrow_sum(row16_sum(s));
      const float mean = s * (1.0f / DM);
      float v = 0.f;
#pragma unroll
      for (int j = 0; j < 8; ++j) { const float d = xs[j] - mean; v = fmaf(d, d, v); }
      v = xrow_sum(row16_sum(v));
      const float var_b = bfr(v * (1.0f / DM));
      const float mean_b = bfr(mean);
      const float den = bfr(sqrtf(bfr(var_b + 1e-5f)));
      const float xn0 = bfr(bfr(bf_lo(xm0) - mean_b) / den);
      const float xn1 = bfr(bfr(bf_lo(xm1) - mean_b) / den);
      xn16[QRW * w + lane] = (uint16_t)(__builtin_bit_cast(unsigned, xn0) >> 16);
      xn16[QRW * w + 64 + lane] = (uint16_t)(__builtin_bit_cast(unsigned, xn1) >> 16);
    }
    u32x2 xa[QR / 4];
#pragma unroll
    for (int i = 0; i < QR / 4; ++i) xa[i] = *reinterpret_cast<const u32x2*>(xn16 + QRW * w + QR * r + 4 * (CFG_QROT ? (i + b) & 7 : i));
    float acc[8];
#pragma unroll
    for (int j = 0; j < 8; ++j) acc[j] = 0.f;
#pragma unroll
    for (int i = 0; i < QR; ++i) {
      const unsigned p = xa[i >> 2][(i >> 1) & 1];
      axpy8(acc, wqv[i], (i & 1) ? (p >> 16) : (p & 0xffffu), (i & 1) ? (p & 0xffff0000u) : (p << 16));
    }
#pragma unroll
    for (int j = 0; j < 8; ++j) acc[j] = xrow_sum(acc[j]);
    if (r == 0) {
      *reinterpret_cast<f32x4*>(&lds[L_QP + w * 128 + 8 * c]) = f32x4{acc[0], acc[1], acc[2], acc[3]};
      *reinterpret_cast<f32x4*>(&lds[L_QP + w * 128 + 8 * c + 4]) = f32x4{acc[4], acc[5], acc[6], acc[7]};
    }
  } else {
    const char* ki = kbase + (size_t)((w - QW) * (CFG_II ? 1 : IL)) * 1024;
#define LDKI(i) KI[i] = ldnt(ki + (i) * (CFG_II ? 4096 : 1024) + loff)
    constexpr int PWN = CFG_PW ? CFG_PW : IL;
#pragma unroll
    for (int i = 0; i < CFG_PRE; ++i) LDKI(i);
    SCHED_FENCE(); __builtin_amdgcn_s_barrier(); SCHED_FENCE();
#pragma unroll
    for (int i = CFG_PRE; i < PWN; ++i) LDKI(i);
#pragma unroll
    for (int i = PWN; i < IL; ++i) {
      SCHED_FENCE(); asm volatile("s_waitcnt vmcnt(%0)" :: "n"(PWN - 1)); SCHED_FENCE();
      LDKI(i);
    }
#undef LDKI
  }
  SCHED_FENCE();
  __syncthreads();
  if (tid < 128) {
    float s = 0.f;
#pragma unroll
    for (int i = 0; i < QW; ++i) s += lds[L_QP + i * 128 + tid];
    reinterpret_cast<uint16_t*>(&lds[L_QB])[tid] = (uint16_t)(__builtin_bit_cast(unsigned, bfr(s)) >> 16);
  }
  SCHED_FENCE();
  __syncthreads();
#define KIDX(i) (4 * IL + ((i) < NK ? NW * (i) : (NW - QW) * NK + QW * (i)) + w)
#define VIDX(i) (NW * (i) + w)
#define LDK(i) ldnt(kbase + (size_t)KIDX(i) * 1024 + loff)
#define LDV(i) ldnt(vbase + (size_t)VIDX(i) * 1024 + loff)
  B0 = LDK(0); B1 = LDK(1); B2 = LDK(2); B3 = LDK(3);
  SCHED_FENCE();
  const u32x4 q = *reinterpret_cast<const u32x4*>(&lds[L_QB + 4 * c]);

  float mx = -INFINITY;
  float* srow = &lds[L_S + r];
#define CONS_K(KR, gc) do { \
    float d_ = dot2(KR[0], q[0], 0.f); d_ = dot2(KR[1], q[1], d_); d_ = dot2(KR[2], q[2], d_); d_ = dot2(KR[3], q[3], d_); \
    d_ = row16_sum(d_); d_ = bfr(bfr(d_) * SCALE) * LOG2E; mx = fmaxf(mx, d_); srow[4 * (gc)] = d_; \
    asm volatile("" : "+v"(mx)); } while (0)
#define STEP_K(it) do { \
    CONS_K(B0, KIDX(it));     SCHED_FENCE(); B0 = LDK((it) + 4); SCHED_FENCE(); \
    CONS_K(B1, KIDX((it) + 1)); SCHED_FENCE(); B1 = LDK((it) + 5); SCHED_FENCE(); \
    CONS_K(B2, KIDX((it) + 2)); SCHED_FENCE(); B2 = LDK((it) + 6); SCHED_FENCE(); \
    CONS_K(B3, KIDX((it) + 3)); SCHED_FENCE(); B3 = LDK((it) + 7); SCHED_FENCE(); } while (0)

  if (w >= QW) {
#pragma unroll
    for (int i = 0; i < IL; ++i) { CONS_K(KI[i], CFG_II ? 4 * i + (w - QW) : (w - QW) * IL + i); if ((i & 3) == 3) SCHED_FENCE(); }
  }
  const int it_w = STAG * (CFG_ROT ? (w + QW) & (NW - 1) : w);
#pragma unroll 1
  for (int it = 0; it < it_w; it += 4) STEP_K(it);
  const int fs = FS * (4 * (h & 7) + (h >> 3));
  const int c0 = fs >> 3;
  const int foff = fs - 8 * c0;
  const int c1 = (fs + FS - 1) >> 3;
  u32x4 gv[FR], uv[FR];
  {
    const unsigned voff = (unsigned)(FR * r8) * (FF * 2) + 16 * min(c0 + c8, c1);
    const char* gb = reinterpret_cast<const char*>(wg) + (size_t)(RW * w) * (FF * 2);
    const char* ub = reinterpret_cast<const char*>(wu) + (size_t)(RW * w) * (FF * 2);
#pragma unroll
    for (int s = 0; s < GG; ++s) {
#pragma unroll
      for (int i = s * (FR / GG); i < (s + 1) * (FR / GG); ++i) { gv[i] = ldw<2>(gb + (size_t)i * (FF * 2) + voff); uv[i] = ldw<2>(ub + (size_t)i * (FF * 2) + voff); }
      SCHED_FENCE();
      STEP_K(it_w + 4 * s);
    }
#pragma unroll
    for (int s = 0; s < GX; ++s) STEP_K(it_w + 4 * (GG + s));
  }
  {
    const u32x4 xa = *reinterpret_cast<const u32x4*>(xn16 + RW * w + FR * r8);
    float ag[8], au[8];
#pragma unroll
    for (int j = 0; j < 8; ++j) { ag[j] = 0.f; au[j] = 0.f; }
#pragma unroll
    for (int i = 0; i < FR; ++i) {
      const unsigned p = xa[i >> 1];
      const unsigned slo = (i & 1) ? (p >> 16) : (p & 0xffffu), shi = (i & 1) ? (p & 0xffff0000u) : (p << 16);
      axpy8(ag, gv[i], slo, shi);
      axpy8(au, uv[i], slo, shi);
    }
#pragma unroll
    for (int j = 0; j < 8; ++j) {
      ag[j] += dpp<ROR8>(ag[j]); ag[j] = xrow_sum(ag[j]);
      au[j] += dpp<ROR8>(au[j]); au[j] = xrow_sum(au[j]);
    }
    if (r8 == 0) {
      float* fp = &lds[L_FP + w * 128 + 8 * c8];
      *reinterpret_cast<f32x4*>(fp) = f32x4{ag[0], ag[1], ag[2], ag[3]};
      *reinterpret_cast<f32x4*>(fp + 4) = f32x4{ag[4], ag[5], ag[6], ag[7]};
      *reinterpret_cast<f32x4*>(fp + 64) = f32x4{au[0], au[1], au[2], au[3]};
      *reinterpret_cast<f32x4*>(fp + 68) = f32x4{au[4], au[5], au[6], au[7]};
    }
  }
  SCHED_FENCE();
  u32x4 wov[OR], wdv[DR];
  const char* db = reinterpret_cast<const char*>(wd) + (size_t)fs * (DM * 2);
#pragma unroll
  for (int i = 0; i < DR; ++i) wdv[i] = ldw<4>(db + (size_t)min(w + NW * i, FS - 1) * (DM * 2) + loff);
  SCHED_FENCE();
  {
    const int n_w = w < QW ? NQ : NK;
    STEP_K(it_w + 4 * (GG + GX));
    int it = it_w + 4 * (GG + GX + 1);
#pragma unroll 1
    do { STEP_K(it); it += 4; } while (it < n_w - 4);
    CONS_K(B0, KIDX(it));     SCHED_FENCE(); B0 = LDV(0); SCHED_FENCE();
    CONS_K(B1, KIDX(it + 1)); SCHED_FENCE(); B1 = LDV(1); SCHED_FENCE();
    CONS_K(B2, KIDX(it + 2)); SCHED_FENCE(); B2 = LDV(2); SCHED_FENCE();
    CONS_K(B3, KIDX(it + 3)); SCHED_FENCE(); B3 = LDV(3); SCHED_FENCE();
  }
  mx = xrow_max(mx);
  if (lane == 0) lds[L_MX + w] = mx;
  MEM_FENCE();
  asm volatile("s_waitcnt lgkmcnt(0)" ::: "memory");
  __builtin_amdgcn_s_barrier();
  MEM_FENCE();
  SCHED_FENCE();
  float M;
  {
    const f32x4 m0 = *reinterpret_cast<const f32x4*>(&lds[L_MX]), m1 = *reinterpret_cast<const f32x4*>(&lds[L_MX + 4]);
    M = fmaxf(fmaxf(fmaxf(m0[0], m0[1]), fmaxf(m0[2], m0[3])), fmaxf(fmaxf(m1[0], m1[1]), fmaxf(m1[2], m1[3])));
  }
  float l = 0.f, acc[8];
#pragma unroll
  for (int j = 0; j < 8; ++j) acc[j] = 0.f;
  float S0 = srow[4 * VIDX(0)], S1 = srow[4 * VIDX(1)], S2 = srow[4 * VIDX(2)], S3 = srow[4 * VIDX(3)];
#define CONS_V(VR, S) do { \
    const float p_ = __builtin_amdgcn_exp2f(S - M); l += p_; \
    _Pragma("unroll") for (int j_ = 0; j_ < 4; ++j_) { \
      acc[2 * j_] = fmaf(p_, bf_lo(VR[j_]), acc[2 * j_]); \
      acc[2 * j_ + 1] = fmaf(p_, bf_hi(VR[j_]), acc[2 * j_ + 1]); } \
    asm volatile("" : "+v"(acc[0]), "+v"(acc[1]), "+v"(acc[2]), "+v"(acc[3]), "+v"(acc[4]), "+v"(acc[5]), "+v"(acc[6]), "+v"(acc[7]), "+v"(l)); \
  } while (0)
#define STEP_V(it) do { \
    CONS_V(B0, S0); SCHED_FENCE(); B0 = LDV((it) + 4); S0 = srow[4 * VIDX((it) + 4)]; SCHED_FENCE(); \
    CONS_V(B1, S1); SCHED_FENCE(); B1 = LDV((it) + 5); S1 = srow[4 * VIDX((it) + 5)]; SCHED_FENCE(); \
    CONS_V(B2, S2); SCHED_FENCE(); B2 = LDV((it) + 6); S2 = srow[4 * VIDX((it) + 6)]; SCHED_FENCE(); \
    CONS_V(B3, S3); SCHED_FENCE(); B3 = LDV((it) + 7); S3 = srow[4 * VIDX((it) + 7)]; SCHED_FENCE(); } while (0)
  const int iv_0 = STAG2 * (w + 1), iv_w = iv_0 + 4 * 0;
  {
    int it = 0;
#pragma unroll 1
    do { STEP_V(it); it += 4; } while (it < iv_0);
  }
  {
    float g = 0.f, u = 0.f;
    const int jr = min(w + NW * (lane & 7), FS - 1);
#pragma unroll
    for (int i = 0; i < NW; ++i) {
      g += lds[L_FP + i * 128 + foff + jr];
      u += lds[L_FP + i * 128 + 64 + foff + jr];
    }
    g = bfr(g); u = bfr(u);
    g = g > 0.f ? g : 0.f;
    const unsigned hb = __builtin_bit_cast(unsigned, bfr(g * u));
    float bb[8];
#pragma unroll
    for (int j = 0; j < 8; ++j) bb[j] = 0.f;
#pragma unroll
    for (int t = 0; t < DR; ++t) {
      unsigned s = __builtin_amdgcn_readlane(hb, t);
      if (w + NW * t >= FS) s = 0u;
      axpy8(bb, wdv[t], s >> 16, s);
    }
    *reinterpret_cast<f32x4*>(&lds[L_RB + w * 512 + 8 * lane]) = f32x4{bb[0], bb[1], bb[2], bb[3]};
    *reinterpret_cast<f32x4*>(&lds[L_RB + w * 512 + 8 * lane + 4]) = f32x4{bb[4], bb[5], bb[6], bb[7]};
  }
  SCHED_FENCE();
  {
    const char* ob = reinterpret_cast<const char*>(wo) + (size_t)(h * DH + OR * w) * (DM * 2);
#pragma unroll
    for (int s = 0; s < OR / WPS; ++s) {
#pragma unroll
      for (int i = 0; i < WPS; ++i) wov[s * WPS + i] = ldw<8>(ob + (s * WPS + i) * (DM * 2) + loff);
      SCHED_FENCE();
      STEP_V(iv_w + 4 * s);
    }
    STEP_V(iv_w + 4 * (OR / WPS));
#pragma unroll
    for (int i = 0; i < OR; i += 2) {
#pragma unroll
      for (int j = 0; j < 4; ++j) {
        const unsigned r0 = wov[i][j], r1 = wov[i + 1][j];
        wov[i][j] = __builtin_amdgcn_perm(r1, r0, 0x05040100u);
        wov[i + 1][j] = __builtin_amdgcn_perm(r1, r0, 0x07060302u);
      }
      asm volatile("" : "+v"(wov[i]), "+v"(wov[i + 1]));
    }
  }
  SCHED_FENCE();
  {
    int it = iv_w + 4 * (OR / WPS + 1);
#pragma unroll 1
    do { STEP_V(it); it += 4; } while (it < NV - 4);
  }
  CONS_V(B0, S0); SCHED_FENCE();
  CONS_V(B1, S1); SCHED_FENCE();
  CONS_V(B2, S2); SCHED_FENCE();
  CONS_V(B3, S3);
#undef STEP_V
#undef CONS_V
#undef STEP_K
#undef CONS_K
#undef LDK
#undef LDV
#undef KIDX
#undef VIDX

  {
    float* rec = &lds[L_REC + (NW * r + w) * 132];
    *reinterpret_cast<f32x4*>(rec + 4 + 8 * c) = f32x4{acc[0], acc[1], acc[2], acc[3]};
    *reinterpret_cast<f32x4*>(rec + 8 + 8 * c) = f32x4{acc[4], acc[5], acc[6], acc[7]};
    if (c == 0) lds[L_QP + NW * r + w] = l;
  }
  SCHED_FENCE();
  __syncthreads();
  SCHED_FENCE();
  float sb = 0.f;
  {
    float ov[NW], bv[NW];
#pragma unroll
    for (int i = 0; i < NW; ++i) ov[i] = lds[L_REC + (NW * r + i) * 132 + 4 + OR * w + c];
    const f32x4 l0 = *reinterpret_cast<const f32x4*>(&lds[L_QP + NW * r]), l1 = *reinterpret_cast<const f32x4*>(&lds[L_QP + NW * r + 4]);
#pragma unroll
    for (int i = 0; i < NW; ++i) bv[i] = lds[L_RB + i * 512 + tid];
    SCHED_FENCE();
    float o = sum8(ov);
    float L = ((l0[0] + l0[2]) + (l1[0] + l1[2])) + ((l0[1] + l0[3]) + (l1[1] + l1[3]));
    sb = sum8(bv);
    L = xrow_sum(L); o = xrow_sum(o);
    const unsigned ob = __builtin_bit_cast(unsigned, bfr(o / L));
    const unsigned nx = __builtin_amdgcn_mov_dpp(ob, 0x101  , 0xf, 0xf, true);
    const unsigned op = __builtin_amdgcn_perm(nx, ob, 0x07060302u);
    float a[8];
#pragma unroll
    for (int j = 0; j < 8; ++j) a[j] = 0.f;
#pragma unroll
    for (int i = 0; i < OR; i += 2) {
      const unsigned s = __builtin_amdgcn_readlane(op, i);
#pragma unroll
      for (int j = 0; j < 4; ++j) { a[2 * j] = dot2(wov[i][j], s, a[2 * j]); a[2 * j + 1] = dot2(wov[i + 1][j], s, a[2 * j + 1]); }
    }
    asm volatile("" : "+v"(sb));
    *reinterpret_cast<f32x4*>(&lds[L_RA + w * 512 + 8 * lane]) = f32x4{a[0], a[1], a[2], a[3]};
    *reinterpret_cast<f32x4*>(&lds[L_RA + w * 512 + 8 * lane + 4]) = f32x4{a[4], a[5], a[6], a[7]};
  }
  __syncthreads();
  {
    float av[NW];
#pragma unroll
    for (int i = 0; i < NW; ++i) av[i] = lds[L_RA + i * 512 + tid];
    SCHED_FENCE();
    const float sa = sum8(av);
    const unsigned ua = __float_as_uint(sa), ub = __float_as_uint(sb);
    const unsigned na = __builtin_amdgcn_mov_dpp(ua, 0x101  , 0xf, 0xf, true);
    const unsigned nb = __builtin_amdgcn_mov_dpp(ub, 0x101, 0xf, 0xf, true);
    const __amdgpu_buffer_rsrc_t pr = __builtin_amdgcn_make_buffer_rsrc(pa + (size_t)(b * NH + h) * (2 * DM), 0, 2 * DM * 4, 0x00020000);
    if (!(lane & 1)) __builtin_amdgcn_raw_buffer_store_b128(u32x4{ua, ub, na, nb}, pr, tid * 8, 0, CFG_SAUX);
  }
}

constexpr int RS = 16;
__global__ void __launch_bounds__(64) reduce_kernel(const uint16_t* __restrict__ x, const float* __restrict__ pp,
                                                    uint16_t* __restrict__ out) {
  const int lane = threadIdx.x, r = lane >> 4, c = lane & 15;
  const int b = blockIdx.x / RS, n0 = (blockIdx.x % RS) * 32 + 2 * c;
  const __amdgpu_buffer_rsrc_t pr = __builtin_amdgcn_make_buffer_rsrc(const_cast<float*>(pp) + (size_t)b * (NH * 2 * DM), 0, NH * 2 * DM * 4, 0x00020000);
  const __amdgpu_buffer_rsrc_t xr = __builtin_amdgcn_make_buffer_rsrc(const_cast<uint16_t*>(x) + b * DM, 0, DM * 2, 0x00020000);
  const __amdgpu_buffer_rsrc_t orr = __builtin_amdgcn_make_buffer_rsrc(out + b * DM, 0, DM * 2, 0x00020000);
  const unsigned voff = (unsigned)(r * 8) * (2 * DM * 4) + n0 * 8;
  u32x4 v[8];
#pragma unroll
  for (int i = 0; i < 8; ++i) v[i] = __builtin_amdgcn_raw_buffer_load_b128(pr, voff, i * (2 * DM * 4), CFG_LAUX);
  unsigned xv = __builtin_amdgcn_raw_buffer_load_b32(xr, n0 * 2, 0, 0);
  asm volatile("" :: "s"(orr));
  SCHED_FENCE();
  float t[4];
#pragma unroll
  for (int j = 0; j < 4; ++j) {
    t[j] = __uint_as_float(v[0][j]);
#pragma unroll
    for (int i = 1; i < 8; ++i) t[j] += __uint_as_float(v[i][j]);
    t[j] = xrow_sum(t[j]);
  }
  asm volatile("" : "+v"(xv));
  const unsigned o0 = __float_as_uint(bfr(bfr(bf_lo(xv) + bfr(t[0])) + bfr(t[1])));
  const unsigned o1 = __float_as_uint(bfr(bfr(bf_hi(xv) + bfr(t[2])) + bfr(t[3])));
  if (r == 0) __builtin_amdgcn_raw_buffer_store_b32((o0 >> 16) | (o1 & 0xffff0000u), orr, n0 * 2, 0, 0);
}

extern "C" void kernel_launch(void* const* d_in, const int* in_sizes, int n_in, void* d_out, int out_size,
                              void* d_ws, size_t ws_size, hipStream_t stream) {
  const uint16_t* x = (const uint16_t*)d_in[0];
  const uint16_t* kc = (const uint16_t*)d_in[1];
  const uint16_t* vc = (const uint16_t*)d_in[2];
  const uint16_t* wq = (const uint16_t*)d_in[3];
  const uint16_t* wo = (const uint16_t*)d_in[4];
  const uint16_t* wg = (const uint16_t*)d_in[6];
  const uint16_t* wu = (const uint16_t*)d_in[7];
  const uint16_t* wd = (const uint16_t*)d_in[8];
  float* pa = (float*)d_ws;
  float* pb = pa + DM;
  fused_decode_kernel<<<NB * NH, NT, 0, stream>>>(x, kc, vc, wq, wo, wg, wu, wd, pa, pb);
  reduce_kernel<<<NB * RS, 64, 0, stream>>>(x, pa, (uint16_t*)d_out);
}
```
